```python
import torch

def _softmax(x, axis=-1):
    return torch.softmax(x, dim=axis)

def _gelu(x):
    return torch.nn.functional.gelu(x, approximate='tanh')

def _silu(x):
    return torch.nn.functional.silu(x)

def _sigmoid(x):
    return torch.sigmoid(x)

def _layernorm(x, axis=-1, eps=1e-05):
    m = x.mean(dim=axis, keepdim=True)
    v = x.var(dim=axis, keepdim=True, unbiased=False)
    return (x - m) / torch.sqrt(v + eps)

def _scatter_add(tgt, idx, src, axis=0):
    return tgt.clone().index_add_(axis, idx, src)

def _segment_sum(src, seg, n):
    out = torch.zeros((n,) + src.shape[1:], dtype=src.dtype)
    return out.index_add_(0, seg, src)

def _pad(x, pw):
    flat = tuple((v for pair in reversed(pw) for v in pair))
    return torch.nn.functional.pad(x, flat)
kernel_name = 'adaLN_cond__compressed_kv_cla__glu_gated_geglu__decode'

def setup_inputs():

    _w = lambda a: (lambda t: t.to(torch.bfloat16) if t.is_floating_point() else t)(torch.as_tensor(a))

    def _A_setup():
        B, H, Sq, Sk, D, M = (4, 32, 1, 4096, 128, 640)
        Dc = 4 * D
        return {'x': _w((lambda *s: torch.randn(*s, dtype=torch.bfloat16) * 0.1)(B, Sq, M)), 'c_kv': _w((lambda *s: torch.randn(*s, dtype=torch.bfloat16) * 6.4)(B, Sk, Dc)), 'w_q': _w((lambda *s: torch.randn(*s, dtype=torch.bfloat16) * 0.8)(M, H, D) * M ** (-0.5)), 'w_uk': _w((lambda *s: torch.randn(*s, dtype=torch.bfloat16) * 0.8)(Dc, H, D) * Dc ** (-0.5)), 'w_uv': _w((lambda *s: torch.randn(*s, dtype=torch.bfloat16) * 6.4)(Dc, H, D) * Dc ** (-0.5)), 'w_o': _w((lambda *s: torch.randn(*s, dtype=torch.bfloat16) * 6.4)(H, D, M) * (H * D) ** (-0.5))}
    _A_in = _A_setup()

    def _B_setup():
        B, S, Dm, Df = (4, 1, 640, 1728)
        return {'x': _w((lambda *s: torch.randn(*s, dtype=torch.bfloat16) * 0.1)(B, S, Dm)), 'w_gate': _w((lambda *s: torch.randn(*s, dtype=torch.bfloat16) * 12.8)(Dm, Df) * Dm ** (-0.5)), 'w_up': _w((lambda *s: torch.randn(*s, dtype=torch.bfloat16) * 12.8)(Dm, Df) * Dm ** (-0.5)), 'w_down': _w((lambda *s: torch.randn(*s, dtype=torch.bfloat16) * 12.8)(Df, Dm) * Df ** (-0.5))}
    _B_in = _B_setup()
    return {'A_x': _A_in['x'], 'A_c_kv': _A_in['c_kv'], 'A_w_q': _A_in['w_q'], 'A_w_uk': _A_in['w_uk'], 'A_w_uv': _A_in['w_uv'], 'A_w_o': _A_in['w_o'], 'B_x': _B_in['x'], 'B_w_gate': _B_in['w_gate'], 'B_w_up': _B_in['w_up'], 'B_w_down': _B_in['w_down'], 'c': _w((lambda *s: torch.randn(*s, dtype=torch.bfloat16) * 0.1)(4, 640) * 8.0), 'w_mod': _w((lambda *s: torch.randn(*s, dtype=torch.bfloat16) * 0.1)(640, 6 * 640) * 640 ** (-0.5) * 8.0)}

def reference(A_x, A_c_kv, A_w_q, A_w_uk, A_w_uv, A_w_o, B_x, B_w_gate, B_w_up, B_w_down, c, w_mod):

    def _A_ref(x, c_kv, w_q, w_uk, w_uv, w_o):
        B, Sq, M = (x.shape[0], x.shape[1], x.shape[2])
        Sk = c_kv.shape[1]
        H, D = (w_q.shape[1], w_q.shape[2])
        q = torch.einsum('bqm,mhd->bhqd', x, w_q)
        k = torch.einsum('bkc,chd->bhkd', c_kv, w_uk)
        v = torch.einsum('bkc,chd->bhkd', c_kv, w_uv)
        scores = torch.einsum('bhqd,bhkd->bhqk', q, k) * D ** (-0.5)
        off = Sk - Sq
        qi = torch.arange(Sq)[:, None]
        kj = torch.arange(Sk)[None, :]
        cmask = kj <= qi + off
        scores = torch.where(cmask[None, None], scores, -1000000000.0)
        p = _softmax(scores, axis=-1)
        ctx = torch.einsum('bhqk,bhkd->bhqd', p, v)
        return torch.einsum('bhqd,hdm->bqm', ctx, w_o)

    def _B_ref(x, w_gate, w_up, w_down):
        g = torch.einsum('bsd,df->bsf', x, w_gate)
        u = torch.einsum('bsd,df->bsf', x, w_up)
        h = _gelu(g) * u
        return torch.einsum('bsf,fd->bsd', h, w_down)
    x = A_x
    mod = (_silu(c) @ w_mod).reshape(c.shape[0], 6, 1, -1)
    sa, ca, ga, sb, cb, gb = (mod[:, 0].clone(), mod[:, 1].clone(), mod[:, 2].clone(), mod[:, 3].clone(), mod[:, 4].clone(), mod[:, 5].clone())
    h = _layernorm(x, axis=-1) * (1 + ca) + sa
    x = x + ga * _A_ref(h, A_c_kv, A_w_q, A_w_uk, A_w_uv, A_w_o)
    h = _layernorm(x, axis=-1) * (1 + cb) + sb
    x = x + gb * _B_ref(h, B_w_gate, B_w_up, B_w_down)
    return x
```

```cpp
#include <hip/hip_runtime.h>
#include <hip/hip_bf16.h>
#include <stdint.h>

typedef unsigned short u16;
__device__ __forceinline__ float bf2f(u16 b) { return __uint_as_float(((unsigned)b) << 16); }
__device__ __forceinline__ u16 f2bf(float f) { unsigned u = __float_as_uint(f); u += 0x7FFFu + ((u >> 16) & 1u); return (u16)(u >> 16); }

constexpr int B = 4, H = 32, SK = 4096, D = 128, M = 640, DC = 512, DF = 1728;

__global__ void gemv_kn(const float* __restrict__ in, int ldin, const u16* __restrict__ W, int K, int N, float* __restrict__ out, int ldout) {
  __shared__ float red[4][4][64];
  int col = blockIdx.x * 64 + (threadIdx.x & 63);
  int ks = threadIdx.x >> 6;
  float acc[4] = {0, 0, 0, 0};
  if (col < N) {
    for (int k = ks; k < K; k += 4) {
      float w = bf2f(W[(size_t)k * N + col]);
      for (int b = 0; b < 4; ++b) acc[b] += in[b * ldin + k] * w;
    }
  }
  for (int b = 0; b < 4; ++b) red[ks][b][threadIdx.x & 63] = acc[b];
  __syncthreads();
  if (ks == 0 && col < N) {
    for (int b = 0; b < 4; ++b) {
      int c = threadIdx.x & 63;
      out[b * ldout + col] = red[0][b][c] + red[1][b][c] + red[2][b][c] + red[3][b][c];
    }
  }
}

__global__ void k_silu(const u16* __restrict__ c, float* __restrict__ out) {
  int i = blockIdx.x * blockDim.x + threadIdx.x;
  if (i < B * M) { float v = bf2f(c[i]); out[i] = v / (1.f + __expf(-v)); }
}

__global__ void k_ln(const u16* __restrict__ xb, const float* __restrict__ xf, const float* __restrict__ mod, int shift_idx, int scale_idx, float* __restrict__ h) {
  int b = blockIdx.x;
  __shared__ float s[256];
  float v[3]; float sum = 0;
  for (int j = 0; j < 3; ++j) { int i = threadIdx.x + j * 256; v[j] = i < M ? (xb ? bf2f(xb[b * M + i]) : xf[b * M + i]) : 0.f; sum += v[j]; }
  s[threadIdx.x] = sum; __syncthreads();
  for (int o = 128; o > 0; o >>= 1) { if (threadIdx.x < o) s[threadIdx.x] += s[threadIdx.x + o]; __syncthreads(); }
  float mean = s[0] / M; __syncthreads();
  float sq = 0;
  for (int j = 0; j < 3; ++j) { int i = threadIdx.x + j * 256; if (i < M) { float d = v[j] - mean; sq += d * d; } }
  s[threadIdx.x] = sq; __syncthreads();
  for (int o = 128; o > 0; o >>= 1) { if (threadIdx.x < o) s[threadIdx.x] += s[threadIdx.x + o]; __syncthreads(); }
  float rstd = rsqrtf(s[0] / M + 1e-5f);
  for (int j = 0; j < 3; ++j) { int i = threadIdx.x + j * 256; if (i < M) h[b * M + i] = (v[j] - mean) * rstd * (1.f + mod[b * 6 * M + scale_idx * M + i]) + mod[b * 6 * M + shift_idx * M + i]; }
}

__global__ void k_qabs(const float* __restrict__ q, const u16* __restrict__ wuk, float* __restrict__ qabs) {
  int h = blockIdx.x, c = blockIdx.y * 64 + (threadIdx.x & 63), b = threadIdx.x >> 6;
  float acc = 0;
  for (int d = 0; d < D; ++d) acc += q[b * H * D + h * D + d] * bf2f(wuk[((size_t)c * H + h) * D + d]);
  qabs[(b * H + h) * DC + c] = acc * 0.08838834764831845f;
}

constexpr int NS = 64, KS = SK / NS;
__global__ void k_attn(const float* __restrict__ qabs, const u16* __restrict__ ckv, float* __restrict__ pm, float* __restrict__ pl, float* __restrict__ po) {
  int b = blockIdx.y, s = blockIdx.x;
  __shared__ float sc[H][KS];
  const u16* kv = ckv + ((size_t)b * SK + s * KS) * DC;
  for (int idx = threadIdx.x; idx < H * KS; idx += 256) {
    int h = idx / KS, k = idx % KS;
    float acc = 0;
    for (int c = 0; c < DC; ++c) acc += qabs[(b * H + h) * DC + c] * bf2f(kv[k * DC + c]);
    sc[h][k] = acc;
  }
  __syncthreads();
  if (threadIdx.x < H) {
    int h = threadIdx.x; float m = -1e30f;
    for (int k = 0; k < KS; ++k) m = fmaxf(m, sc[h][k]);
    float l = 0;
    for (int k = 0; k < KS; ++k) { float p = __expf(sc[h][k] - m); sc[h][k] = p; l += p; }
    pm[(b * NS + s) * H + h] = m; pl[(b * NS + s) * H + h] = l;
  }
  __syncthreads();
  for (int idx = threadIdx.x; idx < H * DC; idx += 256) {
    int h = idx / DC, c = idx % DC;
    float acc = 0;
    for (int k = 0; k < KS; ++k) acc += sc[h][k] * bf2f(kv[k * DC + c]);
    po[((size_t)(b * NS + s) * H + h) * DC + c] = acc;
  }
}

__global__ void k_comb(const float* __restrict__ pm, const float* __restrict__ pl, const float* __restrict__ po, const u16* __restrict__ wuv, float* __restrict__ ctx) {
  int h = blockIdx.x, b = blockIdx.y;
  __shared__ float cc[DC]; __shared__ float w[NS];
  float m = -1e30f;
  for (int s = 0; s < NS; ++s) m = fmaxf(m, pm[(b * NS + s) * H + h]);
  float l = 0;
  for (int s = 0; s < NS; ++s) l += pl[(b * NS + s) * H + h] * __expf(pm[(b * NS + s) * H + h] - m);
  if (threadIdx.x < NS) w[threadIdx.x] = __expf(pm[(b * NS + threadIdx.x) * H + h] - m) / l;
  __syncthreads();
  for (int c = threadIdx.x; c < DC; c += 128) {
    float acc = 0;
    for (int s = 0; s < NS; ++s) acc += w[s] * po[((size_t)(b * NS + s) * H + h) * DC + c];
    cc[c] = acc;
  }
  __syncthreads();
  int d = threadIdx.x;
  float acc = 0;
  for (int c = 0; c < DC; ++c) acc += cc[c] * bf2f(wuv[((size_t)c * H + h) * D + d]);
  ctx[b * H * D + h * D + d] = acc;
}

__global__ void k_res(const u16* __restrict__ xb, const float* __restrict__ xf, const float* __restrict__ mod, int gidx, const float* __restrict__ y, float* __restrict__ outf, u16* __restrict__ outb) {
  int i = blockIdx.x * blockDim.x + threadIdx.x;
  if (i < B * M) {
    int b = i / M, m = i % M;
    float x = xb ? bf2f(xb[i]) : xf[i];
    float r = x + mod[b * 6 * M + gidx * M + m] * y[i];
    if (outf) outf[i] = r;
    if (outb) outb[i] = f2bf(r);
  }
}
__global__ void k_glu(const float* __restrict__ g, const float* __restrict__ u, float* __restrict__ hh) {
  int i = blockIdx.x * blockDim.x + threadIdx.x;
  if (i < B * DF) { float x = g[i]; float t = tanhf(0.7978845608028654f * (x + 0.044715f * x * x * x)); hh[i] = 0.5f * x * (1.f + t) * u[i]; }
}

extern "C" void kernel_launch(void* const* d_in, const int* in_sizes, int n_in, void* d_out, int out_size, void* d_ws, size_t ws_size, hipStream_t stream) {
  const u16* x = (const u16*)d_in[0]; const u16* ckv = (const u16*)d_in[1]; const u16* wq = (const u16*)d_in[2]; const u16* wuk = (const u16*)d_in[3];
  const u16* wuv = (const u16*)d_in[4]; const u16* wo = (const u16*)d_in[5]; const u16* wg = (const u16*)d_in[7]; const u16* wu = (const u16*)d_in[8];
  const u16* wd = (const u16*)d_in[9]; const u16* c = (const u16*)d_in[10]; const u16* wmod = (const u16*)d_in[11];
  float* ws = (float*)d_ws;
  float* sc = ws; ws += 4096; float* mod = ws; ws += 16384; float* h = ws; ws += 4096; float* q = ws; ws += 16384; float* qabs = ws; ws += 65536;
  float* pm = ws; ws += 8192; float* pl = ws; ws += 8192; float* ctx = ws; ws += 16384; float* ao = ws; ws += 4096; float* x2 = ws; ws += 4096; float* h2 = ws; ws += 4096;
  float* g = ws; ws += 8192; float* u = ws; ws += 8192; float* hh = ws; ws += 8192; float* dn = ws; ws += 4096; float* po = ws;
  k_silu<<<10, 256, 0, stream>>>(c, sc);
  gemv_kn<<<60, 256, 0, stream>>>(sc, M, wmod, M, 6 * M, mod, 6 * M);
  k_ln<<<4, 256, 0, stream>>>(x, nullptr, mod, 0, 1, h);
  gemv_kn<<<64, 256, 0, stream>>>(h, M, wq, M, H * D, q, H * D);
  k_qabs<<<dim3(H, DC / 64), 256, 0, stream>>>(q, wuk, qabs);
  k_attn<<<dim3(NS, B), 256, 0, stream>>>(qabs, ckv, pm, pl, po);
  k_comb<<<dim3(H, B), 128, 0, stream>>>(pm, pl, po, wuv, ctx);
  gemv_kn<<<10, 256, 0, stream>>>(ctx, H * D, wo, H * D, M, ao, M);
  k_res<<<10, 256, 0, stream>>>(x, nullptr, mod, 2, ao, x2, nullptr);
  k_ln<<<4, 256, 0, stream>>>(nullptr, x2, mod, 3, 4, h2);
  gemv_kn<<<27, 256, 0, stream>>>(h2, M, wg, M, DF, g, DF);
  gemv_kn<<<27, 256, 0, stream>>>(h2, M, wu, M, DF, u, DF);
  k_glu<<<27, 256, 0, stream>>>(g, u, hh);
  gemv_kn<<<10, 256, 0, stream>>>(hh, DF, wd, DF, M, dn, M);
  k_res<<<10, 256, 0, stream>>>(nullptr, x2, mod, 5, dn, nullptr, (u16*)d_out);
}
```

```cpp
#include <hip/hip_runtime.h>
#include <stdint.h>

typedef unsigned short u16;
typedef unsigned v4u __attribute__((ext_vector_type(4)));
typedef unsigned v2u __attribute__((ext_vector_type(2)));
typedef float f32x4 __attribute__((ext_vector_type(4)));
typedef __bf16 bf16x8 __attribute__((ext_vector_type(8)));
typedef short s16x4 __attribute__((ext_vector_type(4)));
#define GAS __attribute__((address_space(1)))
#define LAS __attribute__((address_space(3)))
#define DEV __device__ __forceinline__

#define ROT 7
#define STAMPS 0

constexpr int NB = 4, NH = 32, SK = 4096, HD = 128, DM = 640, DC = 512, DF = 1728;
constexpr unsigned SENT = 0xFFFFFFFFu;
constexpr unsigned SPIN_CAP = 1u << 16;

constexpr int WS_EPOCH = 0;
constexpr int WS_CF1 = 0x1000;
constexpr int WS_CF2 = 0x2000;
constexpr int WS_HB = 0x100000;
constexpr int WS_QP = 0x10000;
constexpr int WS_QB = 0x30000;
constexpr int WS_ST = 0x50000;
constexpr int WS_PX = 0x60000;
constexpr int WS_PO = 0x80000;
constexpr int WS_MP = 0x90000;
constexpr int WS_PD = 0x120000;
constexpr int WS_OB = 0x200000;
constexpr int WS_STAMP = 0x800000;
constexpr int WS_BYTES = 0x1000000;
constexpr int LDS_BYTES = 163840;

struct Params {
  const u16 *x, *ckv, *wq, *wuk, *wuv, *wo, *wg, *wu, *wd, *c, *wmod;
  u16* out;
  unsigned char* ws;
};

DEV float bf2f(unsigned b) { return __uint_as_float(b << 16); }
DEV float bflo(unsigned w) { return __uint_as_float(w << 16); }
DEV float bfhi(unsigned w) { return __uint_as_float(w & 0xFFFF0000u); }
DEV unsigned pk2(float lo, float hi) {
  typedef __bf16 bf2 __attribute__((ext_vector_type(2)));
  typedef float f2 __attribute__((ext_vector_type(2)));
  f2 v = {lo, hi};
  bf2 r = __builtin_convertvector(v, bf2);
  return __builtin_bit_cast(unsigned, r);
}
DEV unsigned nosent(unsigned v) { return v < 0xFFFFFFFEu ? v : 0xFFFFFFFEu; }
DEV f32x4 mfma32(v4u a, v4u b, f32x4 c) {
  return __builtin_amdgcn_mfma_f32_16x16x32_bf16(__builtin_bit_cast(bf16x8, a), __builtin_bit_cast(bf16x8, b), c, 0, 0, 0);
}
DEV f32x4 mfma16(v2u a, v2u b, f32x4 c) {
  return __builtin_amdgcn_mfma_f32_16x16x16bf16_1k(__builtin_bit_cast(s16x4, a), __builtin_bit_cast(s16x4, b), c, 0, 0, 0);
}
constexpr unsigned PLO = 0x05040100u, PHI = 0x07060302u;
DEV unsigned plo(unsigned hi_row, unsigned lo_row) { return __builtin_amdgcn_perm(hi_row, lo_row, PLO); }
DEV unsigned phi(unsigned hi_row, unsigned lo_row) { return __builtin_amdgcn_perm(hi_row, lo_row, PHI); }
DEV void tr8x8(const v4u (&v)[8], v4u (&f)[8]) {
#pragma unroll
  for (int q = 0; q < 4; ++q) {
#pragma unroll
    for (int t = 0; t < 4; ++t) {
      f[2 * q][t] = plo(v[2 * t + 1][q], v[2 * t][q]);
      f[2 * q + 1][t] = phi(v[2 * t + 1][q], v[2 * t][q]);
    }
  }
}
DEV void tr8x4(const v2u (&v)[8], v4u (&f)[4]) {
#pragma unroll
  for (int q = 0; q < 2; ++q) {
#pragma unroll
    for (int t = 0; t < 4; ++t) {
      f[2 * q][t] = plo(v[2 * t + 1][q], v[2 * t][q]);
      f[2 * q + 1][t] = phi(v[2 * t + 1][q], v[2 * t][q]);
    }
  }
}
DEV void tr8x2(const unsigned (&v)[8], v4u (&f)[2]) {
#pragma unroll
  for (int t = 0; t < 4; ++t) {
    f[0][t] = plo(v[2 * t + 1], v[2 * t]);
    f[1][t] = phi(v[2 * t + 1], v[2 * t]);
  }
}
DEV unsigned tagf(unsigned e) {
  e += 0x9E3779B9u; e ^= e >> 16; e *= 0x85EBCA6Bu; e ^= e >> 13; e *= 0xC2B2AE35u; e ^= e >> 16; return e;
}
#define BAR() asm volatile("s_waitcnt lgkmcnt(0)\n\ts_barrier" ::: "memory")
#define VMWAIT(n) asm volatile("s_waitcnt vmcnt(" #n ")" ::: "memory")
#define FENCE() asm volatile("" ::: "memory")
typedef __amdgpu_buffer_rsrc_t rsrc_t;
DEV v4u ld16s(rsrc_t rs, int off) { return __builtin_amdgcn_raw_buffer_load_b128(rs, off, 0, 16); }
DEV v2u ld8s(rsrc_t rs, int off) { return __builtin_amdgcn_raw_buffer_load_b64(rs, off, 0, 16); }
DEV unsigned ld4s(rsrc_t rs, int off) { return __builtin_amdgcn_raw_buffer_load_b32(rs, off, 0, 16); }
DEV void st16s(rsrc_t rs, int off, v4u v) { __builtin_amdgcn_raw_buffer_store_b128(v, rs, off, 0, 16); }
DEV void st8s(rsrc_t rs, int off, v2u v) { __builtin_amdgcn_raw_buffer_store_b64(v, rs, off, 0, 16); }
DEV void st4s(rsrc_t rs, int off, unsigned v) { __builtin_amdgcn_raw_buffer_store_b32(v, rs, off, 0, 16); }
DEV bool ok16(v4u v) {
  unsigned m = max(max(v.x, v.y), max(v.z, v.w));
  return m != SENT;
}
DEV bool ok8(v2u v) { return max(v.x, v.y) != SENT; }
template <typename T> DEV T ldg(const void* p) { return *(const GAS T*)p; }
template <typename T> DEV T ldnt(const void* p) { return __builtin_nontemporal_load((const GAS T*)p); }
template <typename T> DEV T lds_ld(unsigned off) { return *(const LAS T*)(uintptr_t)off; }
template <typename T> DEV void lds_st(unsigned off, T v) { *(LAS T*)(uintptr_t)off = v; }

DEV float dpp_xor1(float v) { return __builtin_bit_cast(float, __builtin_amdgcn_mov_dpp(__builtin_bit_cast(int, v), 0xB1, 0xf, 0xf, true)); }
DEV float dpp_xor2(float v) { return __builtin_bit_cast(float, __builtin_amdgcn_mov_dpp(__builtin_bit_cast(int, v), 0x4E, 0xf, 0xf, true)); }
DEV float dpp_ror4(float v) { return __builtin_bit_cast(float, __builtin_amdgcn_mov_dpp(__builtin_bit_cast(int, v), 0x124, 0xf, 0xf, true)); }
DEV float dpp_ror8(float v) { return __builtin_bit_cast(float, __builtin_amdgcn_mov_dpp(__builtin_bit_cast(int, v), 0x128, 0xf, 0xf, true)); }
DEV float sum16(float v) { v += dpp_xor1(v); v += dpp_xor2(v); v += dpp_ror4(v); v += dpp_ror8(v); return v; }
DEV float x16_sum(float x) {
  auto s = __builtin_amdgcn_permlane16_swap(__float_as_uint(x), __float_as_uint(x), false, false);
  return __uint_as_float(s[0]) + __uint_as_float(s[1]);
}
DEV float x32_sum(float x) {
  auto t = __builtin_amdgcn_permlane32_swap(__float_as_uint(x), __float_as_uint(x), false, false);
  return __uint_as_float(t[0]) + __uint_as_float(t[1]);
}
DEV float x16_max(float x) {
  auto s = __builtin_amdgcn_permlane16_swap(__float_as_uint(x), __float_as_uint(x), false, false);
  return fmaxf(__uint_as_float(s[0]), __uint_as_float(s[1]));
}
DEV float x32_max(float x) {
  auto t = __builtin_amdgcn_permlane32_swap(__float_as_uint(x), __float_as_uint(x), false, false);
  return fmaxf(__uint_as_float(t[0]), __uint_as_float(t[1]));
}
DEV float max16(float v) { v = fmaxf(v, dpp_xor1(v)); v = fmaxf(v, dpp_xor2(v)); v = fmaxf(v, dpp_ror4(v)); v = fmaxf(v, dpp_ror8(v)); return v; }
DEV f32x4 sel4(bool c, f32x4 a, f32x4 b) { f32x4 r; r[0] = c ? a[0] : b[0]; r[1] = c ? a[1] : b[1]; r[2] = c ? a[2] : b[2]; r[3] = c ? a[3] : b[3]; return r; }
DEV v2u sel2(bool c, v2u a, v2u b) { v2u r; r[0] = c ? a[0] : b[0]; r[1] = c ? a[1] : b[1]; return r; }
DEV bool wall(bool p) { return __builtin_amdgcn_ballot_w64(!p) == 0ull; }

#define STAMP(i) do { } while (0)
#define FLUSH_STAMPS() do { } while (0)
DEV rsrc_t mkrs(const void* ptr, int bytes) { return __builtin_amdgcn_make_buffer_rsrc((void*)ptr, 0, bytes, 0x00020000); }
DEV v4u bl16(rsrc_t r, int voff, int soff) { return __builtin_amdgcn_raw_buffer_load_b128(r, voff, soff, 0); }
DEV v4u bl16nt(rsrc_t r, int voff, int soff) { return __builtin_amdgcn_raw_buffer_load_b128(r, voff, soff, 2); }
DEV v2u bl8(rsrc_t r, int voff, int soff) { return __builtin_amdgcn_raw_buffer_load_b64(r, voff, soff, 0); }
DEV unsigned bl4(rsrc_t r, int voff, int soff) { return __builtin_amdgcn_raw_buffer_load_b32(r, voff, soff, 0); }
DEV unsigned bad16(v4u v) { return (unsigned)(max(max(v.x, v.y), max(v.z, v.w)) == SENT); }
DEV bool wnone(unsigned bad) { return __builtin_amdgcn_ballot_w64(bad != 0u) == 0ull; }
DEV float silu(float v) { return v * __builtin_amdgcn_rcpf(1.f + __expf(-v)); }
DEV v4u silu_frag(v4u cv, bool lo_lane) {
  v4u o;
#pragma unroll
  for (int q = 0; q < 4; ++q) {
    const float a = silu(bflo(cv[q])), b = silu(bfhi(cv[q]));
    const unsigned hi = pk2(a, b);
    const unsigned lo = pk2(a - bflo(hi), b - bfhi(hi));
    o[q] = lo_lane ? lo : hi;
  }
  return o;
}

__global__ void __launch_bounds__(512, 2) mega(Params p) {
  extern __shared__ __attribute__((aligned(16))) unsigned char smem_[];
  STAMP(0);
  const int tid = threadIdx.x, lane = tid & 63;
  const int w = __builtin_amdgcn_readfirstlane(tid >> 6);
  const int x8 = blockIdx.x & 7, slot = blockIdx.x >> 3;
  const int xg = (x8 + ROT) & 7;
  const rsrc_t rs = mkrs(p.ws, WS_BYTES);
  const int l15 = lane & 15, g = lane >> 4;
  const unsigned epoch = *(const __attribute__((address_space(4))) unsigned*)(p.ws + WS_EPOCH);
  const unsigned tag = tagf(epoch);
  const v4u sent4 = {SENT, SENT, SENT, SENT};
  const f32x4 zero4 = {0.f, 0.f, 0.f, 0.f};

  if (xg < 4) {
    const int h = xg * 8 + (slot >> 2), q4 = slot & 3;
    const bool isA = slot < 20;
    const int r = xg * 20 + slot;
    constexpr unsigned L_RA = 0;
    constexpr unsigned L_HA = 8192;
    constexpr unsigned L_RB = 16384;
    constexpr unsigned L_W = 28672;
    constexpr unsigned L_CC = 30720;
    constexpr unsigned L_RF = 36864;
    constexpr unsigned L_R2 = 40960;
    const rsrc_t rs_x = mkrs(p.x, NB * DM * 2);
    const rsrc_t rs_c = mkrs(p.c, NB * DM * 2);
    const rsrc_t rs_wmod = mkrs(p.wmod, DM * 6 * DM * 2);

    v4u fq[8];
    if (w < 5) {
      const rsrc_t rs_wq = mkrs(p.wq, DM * NH * HD * 2);
      const int qoff = ((160 * q4 + 32 * w + 8 * g) * (NH * HD) + HD * h + 8 * l15) * 2;
      v4u vq[8];
      if (isA) {
        const v4u cf = bl16(rs_c, ((l15 >> 2) * DM + 160 * xg + 32 * w + 8 * g) * 2, 0);
        v2u va[8];
        const int aoff = ((160 * xg + 32 * w + 8 * g) * (6 * DM) + 64 * slot + 4 * l15) * 2;
#pragma unroll
        for (int i = 0; i < 8; ++i) va[i] = bl8(rs_wmod, aoff, i * (6 * DM * 2));
#pragma unroll
        for (int i = 0; i < 8; ++i) vq[i] = bl16nt(rs_wq, qoff, i * (NH * HD * 2));
        const v4u af = silu_frag(cf, lane & 1);
        v4u fa[4];
        tr8x4(va, fa);
        f32x4 o;
#pragma unroll
        for (int j = 0; j < 4; ++j) { const f32x4 d = mfma32(af, fa[j], zero4); o[j] = d[0] + d[1]; }
        lds_st<f32x4>(L_RA + ((w * 4 + g) * 64 + 4 * l15) * 4, o);
        STAMP(1);
        BAR();
        if (w < 2) {
          const int b = tid >> 5, pr = tid & 31;
          float s0 = 0.f, s1 = 0.f;
#pragma unroll
          for (int ww = 0; ww < 5; ++ww) {
            const v2u t2 = lds_ld<v2u>(L_RA + ((ww * 4 + b) * 64 + 2 * pr) * 4);
            s0 += __uint_as_float(t2.x); s1 += __uint_as_float(t2.y);
          }
          const int part = slot >= 10, cbk = slot - 10 * part;
          st8s(rs, WS_HB + (((part * 4 + xg) * 4 + b) * 320 + 32 * cbk + pr) * 8, v2u{pk2(s0, s1), tag});
        }
        STAMP(2);
      } else {
#pragma unroll
        for (int i = 0; i < 8; ++i) vq[i] = bl16nt(rs_wq, qoff, i * (NH * HD * 2));
      }
      if (w == 4) {
        unsigned spins = 0;
        for (;;) {
          FENCE();
          const v4u v = ld16s(rs, WS_CF1 + lane * 16);
          if (wnone((v.x != tag) | (v.y != tag) | (v.z != tag) | (v.w != tag)) || ++spins > SPIN_CAP) break;
          __builtin_amdgcn_s_sleep(2);
        }
      }
      tr8x8(vq, fq);
    } else {
      const int v3 = w - 5;
      const int mq = min(16 * v3 + l15, 39);
      if (w == 7) {
        {
          const int seg = lane >> 2, ks = seg >> 2, b = seg & 3, pc = lane & 3;
          st16s(rs, WS_QP + h * 4096 + ks * 1024 + (4 * b + q4) * 64 + pc * 16, sent4);
          st16s(rs, WS_PX + xg * 32768 + (4 * (h & 7) + ks) * 1024 + (4 * b + q4) * 64 + pc * 16, sent4);
        }
        {
          const int chi = 16 * q4 + l15, ch2 = chi >> 5, c5 = chi & 31, s = c5 & 7, t = c5 >> 3, gg = ((t & 1) << 1) | (t >> 1);
          st16s(rs, WS_QB + g * 32768 + ch2 * 16384 + s * 2048 + (h >> 4) * 1024 + gg * 256 + (h & 15) * 16, sent4);
        }
        if (isA) {
          if (lane < 32) st16s(rs, WS_PO + (xg * 4 + (lane >> 3)) * 2560 + slot * 128 + (lane & 7) * 16, sent4);
        } else {
          const int mi = xg * 12 + (slot - 20);
          if (mi < 40) st16s(rs, WS_MP + ((mi / 10) * 4 + g) * 2560 + (mi % 10) * 256 + l15 * 16, sent4);
        }
      }
      v4u xs[5];
#pragma unroll
      for (int it = 0; it < 5; ++it) xs[it] = bl16(rs_x, (g * DM + 8 * l15) * 2, it * 256);
      const v2u xq = bl8(rs_x, (g * DM + 160 * q4 + 4 * mq) * 2, 0);
      float s1 = 0.f, s2 = 0.f;
#pragma unroll
      for (int it = 0; it < 5; ++it)
#pragma unroll
        for (int q = 0; q < 4; ++q) { const float a = bflo(xs[it][q]), b2 = bfhi(xs[it][q]); s1 += a + b2; s2 += a * a + b2 * b2; }
      s1 = sum16(s1); s2 = sum16(s2);
      const float mean = s1 * (1.f / DM);
      const float rstd = rsqrtf(fmaxf(s2 * (1.f / DM) - mean * mean, 0.f) + 1e-5f);
      float xn[4] = {(bflo(xq.x) - mean) * rstd, (bfhi(xq.x) - mean) * rstd, (bflo(xq.y) - mean) * rstd, (bfhi(xq.y) - mean) * rstd};
      if (w == 7) {
        VMWAIT(0);
        if (lane == 0) st4s(rs, WS_CF1 + blockIdx.x * 4, tag);
      }
      if (isA) BAR();
      v4u hv[8];
      const int hoff = WS_HB + (g * 320 + 80 * q4 + 2 * mq) * 8;
      unsigned spins = 0;
      for (;;) {
        FENCE();
        unsigned bad = 0;
#pragma unroll
        for (int i = 0; i < 8; ++i) hv[i] = __builtin_amdgcn_raw_buffer_load_b128(rs, hoff, i * 10240, 16);
#pragma unroll
        for (int i = 0; i < 8; ++i) bad |= (hv[i].y ^ tag) | (hv[i].w ^ tag);
        if (wnone(bad) || ++spins > SPIN_CAP) break;
        __builtin_amdgcn_s_sleep(1);
      }
      float hh[4];
#pragma unroll
      for (int e = 0; e < 4; ++e) {
        float sa = 0.f, ca = 1.f;
#pragma unroll
        for (int kq = 0; kq < 4; ++kq) {
          const unsigned ws_ = hv[kq][(e >> 1) * 2], wc_ = hv[4 + kq][(e >> 1) * 2];
          sa += (e & 1) ? bfhi(ws_) : bflo(ws_);
          ca += (e & 1) ? bfhi(wc_) : bflo(wc_);
        }
        hh[e] = xn[e] * ca + sa;
      }
      const unsigned h01 = pk2(hh[0], hh[1]), h23 = pk2(hh[2], hh[3]);
      const unsigned l01 = pk2(hh[0] - bflo(h01), hh[1] - bfhi(h01)), l23 = pk2(hh[2] - bflo(h23), hh[3] - bfhi(h23));
      lds_st<v2u>(L_HA + (4 * g + 0) * 336 + mq * 8, v2u{h01, h23});
      lds_st<v2u>(L_HA + (4 * g + 1) * 336 + mq * 8, v2u{l01, l23});
    }
    STAMP(3);
    v4u vk[4];
    {
      const rsrc_t rs_wuk = mkrs(p.wuk, DC * NH * HD * 2);
      const int off = (((128 * q4 + 16 * w + l15) * NH + h) * HD + 8 * g) * 2;
#pragma unroll
      for (int ks = 0; ks < 4; ++ks) vk[ks] = bl16(rs_wuk, off, ks * 64);
    }
    BAR();
    STAMP(4);
    if (w < 5) {
      const v4u a = lds_ld<v4u>(L_HA + l15 * 336 + (32 * w + 8 * g) * 2);
      float o[8];
#pragma unroll
      for (int j = 0; j < 8; ++j) { const f32x4 d = mfma32(a, fq[j], zero4); o[j] = d[0] + d[1]; }
      const unsigned off = L_RB + ((w * 4 + g) * 128 + 8 * l15) * 4;
      lds_st<f32x4>(off, f32x4{o[0], o[1], o[2], o[3]});
      lds_st<f32x4>(off + 16, f32x4{o[4], o[5], o[6], o[7]});
    }
    BAR();
    if (w < 4) {
      const int dp = lane;
      float s0 = 0.f, s1 = 0.f;
#pragma unroll
      for (int ww = 0; ww < 5; ++ww) {
        const v2u t2 = lds_ld<v2u>(L_RB + ((ww * 4 + w) * 128 + 2 * dp) * 4);
        s0 += __uint_as_float(t2.x); s1 += __uint_as_float(t2.y);
      }
      st4s(rs, WS_QP + h * 4096 + (dp >> 4) * 1024 + (4 * w + q4) * 64 + (dp & 15) * 4, nosent(pk2(s0, s1)));
    }
    STAMP(5);
    {
      const int cl = 16 * w + l15;
      const int chi = 16 * q4 + (cl >> 3), ch2 = chi >> 5, c5 = chi & 31, s = c5 & 7, t = c5 >> 3, gg = ((t & 1) << 1) | (t >> 1);
      const int qboff = WS_QB + g * 32768 + ch2 * 16384 + s * 2048 + (h >> 4) * 1024 + gg * 256 + (h & 15) * 16 + (cl & 7) * 2;
      const int qpoff = WS_QP + h * 4096 + l15 * 64 + g * 16;
      v4u qa[4];
      unsigned spins = 0;
      for (;;) {
        FENCE();
#pragma unroll
        for (int ks = 0; ks < 4; ++ks) qa[ks] = __builtin_amdgcn_raw_buffer_load_b128(rs, qpoff + ks * 1024, 0, 16);
        if (wnone(bad16(qa[0]) | bad16(qa[1]) | bad16(qa[2]) | bad16(qa[3])) || ++spins > SPIN_CAP) break;
        __builtin_amdgcn_s_sleep(1);
      }
      STAMP(6);
      f32x4 d = zero4;
#pragma unroll
      for (int ks = 0; ks < 4; ++ks) d = mfma32(qa[ks], vk[ks], d);
      const float qv = ((d[0] + d[1]) + (d[2] + d[3])) * (0.08838834764831845f * 1.4426950408889634f);
      const float nb = dpp_xor1(qv);
      if (!(lane & 1)) st4s(rs, qboff, nosent(pk2(qv, nb)));
    }
    STAMP(7);
    const int obase = WS_OB + (h * 4 + q4) * 32768;
#pragma unroll
    for (int i = 0; i < 4; ++i) st16s(rs, obase + (w * 4 + i) * 1024 + lane * 16, sent4);
    v4u fe[4][2];
    v4u ff[4][2];
    if (w < 4) {
      const rsrc_t rs_wuv = mkrs(p.wuv, DC * NH * HD * 2);
      unsigned ve[4][8];
      const int off = (((128 * q4 + 4 * (g >> 1) + 16 * (g & 1)) * NH + h) * HD + 32 * w + 2 * l15) * 2;
#pragma unroll
      for (int ks = 0; ks < 4; ++ks)
#pragma unroll
        for (int i = 0; i < 8; ++i) ve[ks][i] = bl4(rs_wuv, off, (32 * ks + 8 * (i >> 2) + (i & 3)) * (NH * HD * 2));
      VMWAIT(32);
      BAR();
#pragma unroll
      for (int ks = 0; ks < 4; ++ks) tr8x2(ve[ks], fe[ks]);
    } else {
      VMWAIT(0);
      BAR();
      if (tid == 448) st4s(rs, WS_CF2 + (xg * 32 + slot) * 4, tag);
    }
    if (isA) {
      const rsrc_t rs_wo = mkrs(p.wo, NH * HD * DM * 2);
      unsigned vf[4][8];
      const int off = ((1024 * xg + 128 * w + 8 * g) * DM + 32 * slot + 2 * l15) * 2;
#pragma unroll
      for (int ks = 0; ks < 4; ++ks)
#pragma unroll
        for (int i = 0; i < 8; ++i) vf[ks][i] = bl4(rs_wo, off, (32 * ks + i) * (DM * 2));
#pragma unroll
      for (int ks = 0; ks < 4; ++ks) tr8x2(vf[ks], ff[ks]);
    } else {
      const int mi = xg * 12 + (slot - 20);
      if (mi < 40) {
        const int pidx = mi / 10, cbk = mi % 10;
        const int mcol = (pidx == 0 ? 2 : (pidx == 1 ? 5 : (pidx == 2 ? 3 : 4))) * DM + 64 * cbk;
        f32x4 ac[4] = {zero4, zero4, zero4, zero4};
#pragma unroll
        for (int n = 0; n < 3; ++n) {
          const int kb = w + 8 * n;
          if (kb < 20) {
            const v4u cf = bl16(rs_c, ((l15 >> 2) * DM + 32 * kb + 8 * g) * 2, 0);
            v2u va[8];
            const int aoff = ((32 * kb + 8 * g) * (6 * DM) + mcol + 4 * l15) * 2;
#pragma unroll
            for (int i = 0; i < 8; ++i) va[i] = bl8(rs_wmod, aoff, i * (6 * DM * 2));
            const v4u af = silu_frag(cf, lane & 1);
            v4u fa[4];
            tr8x4(va, fa);
#pragma unroll
            for (int j = 0; j < 4; ++j) ac[j] = mfma32(af, fa[j], ac[j]);
          }
        }
        lds_st<f32x4>(L_RA + ((w * 4 + g) * 64 + 4 * l15) * 4, f32x4{ac[0][0] + ac[0][1], ac[1][0] + ac[1][1], ac[2][0] + ac[2][1], ac[3][0] + ac[3][1]});
        BAR();
        if (w < 4) {
          float s = pidx == 3 ? 1.f : 0.f;
#pragma unroll
          for (int ww = 0; ww < 8; ++ww) s += lds_ld<float>(L_RA + ((ww * 4 + w) * 64 + lane) * 4);
          st4s(rs, WS_MP + (pidx * 4 + w) * 2560 + (64 * cbk + lane) * 4, nosent(__float_as_uint(s)));
        }
      }
    }
    STAMP(8);
    {
      const int b = w >> 1, sh = w & 1;
      unsigned spins = 0;
      v2u st;
      const int stoff = WS_ST + ((h * 4 + b) * 32 + (lane & 31)) * 8;
      const int ooff = obase + (b * 32 + 16 * sh + 4 * g) * 256 + l15 * 16;
      for (;;) {
        FENCE();
        st = ld8s(rs, stoff);
        if (wnone((unsigned)(max(st.x, st.y) == SENT)) || ++spins > SPIN_CAP) break;
        __builtin_amdgcn_s_sleep(2);
      }
      STAMP(9);
      v4u o[4];
#pragma unroll
      for (int i = 0; i < 4; ++i) o[i] = __builtin_amdgcn_raw_buffer_load_b128(rs, ooff + i * 256, 0, 16);
      const float m = __uint_as_float(st.x), l = __uint_as_float(st.y);
      const float M = x16_max(max16(m));
      const float e = __builtin_amdgcn_exp2f(m - M);
      const float L = x16_sum(sum16(l * e));
      const float wt = e * __builtin_amdgcn_rcpf(L);
      lds_st<float>(L_W + w * 128 + (lane & 31) * 4, wt);
      const f32x4 w4 = lds_ld<f32x4>(L_W + w * 128 + (16 * sh + 4 * g) * 4);
      v2u aw;
      {
        const unsigned h01 = pk2(w4[0], w4[1]), h23 = pk2(w4[2], w4[3]);
        const unsigned l01 = pk2(w4[0] - bflo(h01), w4[1] - bfhi(h01)), l23 = pk2(w4[2] - bflo(h23), w4[3] - bfhi(h23));
        aw = sel2(l15 == 0, v2u{h01, h23}, sel2(l15 == 1, v2u{l01, l23}, v2u{0u, 0u}));
      }
      while (!wnone(bad16(o[0]) | bad16(o[1]) | bad16(o[2]) | bad16(o[3])) && ++spins <= SPIN_CAP) {
        FENCE();
#pragma unroll
        for (int i = 0; i < 4; ++i) o[i] = __builtin_amdgcn_raw_buffer_load_b128(rs, ooff + i * 256, 0, 16);
      }
      STAMP(10);
      float cc[8];
#pragma unroll
      for (int q = 0; q < 4; ++q) {
        const v2u b0 = {plo(o[1][q], o[0][q]), plo(o[3][q], o[2][q])};
        const v2u b1 = {phi(o[1][q], o[0][q]), phi(o[3][q], o[2][q])};
        const f32x4 d0 = mfma16(aw, b0, zero4);
        const f32x4 d1 = mfma16(aw, b1, zero4);
        cc[2 * q] = d0[0] + d0[1];
        cc[2 * q + 1] = d1[0] + d1[1];
      }
      if (lane < 16) lds_st<v4u>(L_CC + (4 * b + sh) * 272 + lane * 16, v4u{pk2(cc[0], cc[1]), pk2(cc[2], cc[3]), pk2(cc[4], cc[5]), pk2(cc[6], cc[7])});
      BAR();
      if (w < 4) {
        f32x4 d0 = zero4, d1 = zero4;
        v4u a[4];
#pragma unroll
        for (int ks = 0; ks < 4; ++ks) a[ks] = lds_ld<v4u>(L_CC + l15 * 272 + (32 * ks + 8 * g) * 2);
#pragma unroll
        for (int ks = 0; ks < 4; ++ks) { d0 = mfma32(a[ks], fe[ks][0], d0); d1 = mfma32(a[ks], fe[ks][1], d1); }
        st4s(rs, WS_PX + xg * 32768 + (4 * (h & 7) + w) * 1024 + (4 * g + q4) * 64 + l15 * 4, nosent(pk2(d0[0] + d0[1], d1[0] + d1[1])));
      }
    }
    STAMP(11);
    if (!isA) { FLUSH_STAMPS(); return; }
    {
      v4u pa[4];
      unsigned spins = 0;
      const int poff = WS_PX + xg * 32768 + (4 * w) * 1024 + l15 * 64 + g * 16;
      for (;;) {
        FENCE();
#pragma unroll
        for (int ks = 0; ks < 4; ++ks) pa[ks] = __builtin_amdgcn_raw_buffer_load_b128(rs, poff + ks * 1024, 0, 16);
        if (wnone(bad16(pa[0]) | bad16(pa[1]) | bad16(pa[2]) | bad16(pa[3])) || ++spins > SPIN_CAP) break;
        __builtin_amdgcn_s_sleep(1);
      }
      STAMP(12);
      f32x4 d0 = zero4, d1 = zero4;
#pragma unroll
      for (int ks = 0; ks < 4; ++ks) { d0 = mfma32(pa[ks], ff[ks][0], d0); d1 = mfma32(pa[ks], ff[ks][1], d1); }
      const float o0 = (d0[0] + d0[1]) + (d0[2] + d0[3]), o1 = (d1[0] + d1[1]) + (d1[2] + d1[3]);
      lds_st<v2u>(L_RF + ((w * 4 + g) * 32 + 2 * l15) * 4, v2u{__float_as_uint(o0), __float_as_uint(o1)});
      BAR();
      if (w < 2) {
        const int b = tid >> 5, cl = tid & 31;
        float s = 0.f;
#pragma unroll
        for (int ww = 0; ww < 8; ++ww) s += lds_ld<float>(L_RF + ((ww * 4 + b) * 32 + cl) * 4);
        st4s(rs, WS_PO + (xg * 4 + b) * 2560 + (32 * slot + cl) * 4, nosent(__float_as_uint(s)));
      }
    }
    STAMP(13);
    float r2_x = 0.f, r2_gb = 0.f;
    if (w == 7) {
      const int b = (lane >> 3) & 3, j = lane & 7;
      unsigned spins = 0;
      unsigned a0, a1, a2, a3, ga, gb;
      const int o1 = (b * 640 + 8 * r + j) * 4;
      const unsigned xr = __builtin_amdgcn_raw_buffer_load_b16(rs_x, (b * DM + 8 * r + j) * 2, 0, 0);
      for (;;) {
        FENCE();
        a0 = ld4s(rs, WS_PO + 0 * 10240 + o1);
        a1 = ld4s(rs, WS_PO + 1 * 10240 + o1);
        a2 = ld4s(rs, WS_PO + 2 * 10240 + o1);
        a3 = ld4s(rs, WS_PO + 3 * 10240 + o1);
        ga = ld4s(rs, WS_MP + 0 * 10240 + o1);
        gb = ld4s(rs, WS_MP + 1 * 10240 + o1);
        if (wnone((unsigned)(max(max(max(a0, a1), max(a2, a3)), max(ga, gb)) == SENT)) || ++spins > SPIN_CAP) break;
        __builtin_amdgcn_s_sleep(2);
      }
      r2_gb = __uint_as_float(gb);
      r2_x = bf2f(xr) + __uint_as_float(ga) * ((__uint_as_float(a0) + __uint_as_float(a1)) + (__uint_as_float(a2) + __uint_as_float(a3)));
    } else {
      unsigned spins = 0;
      v4u t4;
      const int pdoff = WS_PD + r * 6912 + min(tid, 431) * 16;
      for (;;) {
        FENCE();
        t4 = ld16s(rs, pdoff);
        if (wnone(bad16(t4)) || ++spins > SPIN_CAP) break;
        __builtin_amdgcn_s_sleep(1);
      }
      f32x4 v = __builtin_bit_cast(f32x4, t4);
      if (tid >= 432) v = zero4;
#pragma unroll
      for (int e = 0; e < 4; ++e) { float s = v[e]; s += dpp_ror8(s); s = x16_sum(s); s = x32_sum(s); v[e] = s; }
      if (lane < 8) lds_st<f32x4>(L_R2 + (w * 8 + lane) * 16, v);
    }
    STAMP(14);
    BAR();
    if (w == 7 && lane < 32) {
      const int b = lane >> 3, j = lane & 7;
      float s = 0.f;
#pragma unroll
      for (int ww = 0; ww < 7; ++ww) s += lds_ld<float>(L_R2 + ((ww * 8 + b * 2 + (j >> 2)) * 4 + (j & 3)) * 4);
      const float o = r2_x + r2_gb * s;
      *(GAS u16*)(p.out + b * DM + 8 * r + j) = (u16)(pk2(o, 0.f) & 0xFFFFu);
    }
    if (r == 0 && tid == 0) st4s(rs, WS_EPOCH, epoch + 1u);
    STAMP(15);
    FLUSH_STAMPS();
  } else {
    const int b = xg - 4, s = slot;
    const int gp = b * 14 + slot;
    const bool isG = slot < 14 && gp < 54;
    constexpr unsigned L_KV = 0, L_Q = 131072;
    constexpr unsigned L_EX = L_Q;
    constexpr unsigned L_MX = L_Q + 16384;
    constexpr unsigned L_SM = L_Q + 17408;
    constexpr unsigned L_P = L_Q + 20480;
    if (w == 7) {
      if (lane < 32) st8s(rs, WS_ST + ((lane * 4 + b) * 32 + s) * 8, v2u{SENT, SENT});
      if (isG) {
        const int o0 = WS_PD + (lane >> 3) * 6912 + gp * 128 + (lane & 7) * 16;
#pragma unroll
        for (int i = 0; i < 10; ++i) __builtin_amdgcn_raw_buffer_store_b128(sent4, rs, o0, i * 8 * 6912, 16);
      }
    }
    {
      const u16* kvb = p.ckv + ((size_t)b * SK + s * 128 + 16 * w) * DC;
#pragma unroll
      for (int i = 0; i < 16; ++i) {
        const u16* src = kvb + (size_t)i * DC + ((lane ^ i) * 8);
        unsigned keep;
        asm volatile("s_mov_b32 %0, m0\n\ts_mov_b32 m0, %2\n\ts_nop 0\n\tglobal_load_lds_dwordx4 %1, off nt\n\ts_mov_b32 m0, %0"
                     : "=&s"(keep) : "v"(src), "s"(L_KV + (16 * w + i) * 1024) : "memory");
      }
    }
    if (w == 7) {
      VMWAIT(16);
      if (lane == 0) st4s(rs, WS_CF1 + blockIdx.x * 4, tag);
    }
    VMWAIT(0);
    STAMP(1);
    if (w == 7) {
      unsigned spins = 0;
      for (;;) {
        FENCE();
        const v4u v = ld16s(rs, WS_CF1 + lane * 16);
        if (wnone((v.x != tag) | (v.y != tag) | (v.z != tag) | (v.w != tag)) || ++spins > SPIN_CAP) break;
        __builtin_amdgcn_s_sleep(2);
      }
    }
    BAR();
    const int t = w >> 1, ch = w & 1;
    const int goff = (g == 0) ? 0 : (g == 1 ? 16 : (g == 2 ? 8 : 24));
    v4u kf[8][2];
#pragma unroll
    for (int ss = 0; ss < 8; ++ss)
#pragma unroll
      for (int kt = 0; kt < 2; ++kt)
        kf[ss][kt] = lds_ld<v4u>(L_KV + (32 * t + 16 * kt + l15) * 1024 + (((32 * ch + ss + goff) ^ l15) * 16));
    {
      unsigned spins = 0;
      v4u q[4];
      const int qoff = WS_QB + b * 32768 + w * 4096 + lane * 16;
      for (;;) {
        FENCE();
#pragma unroll
        for (int i = 0; i < 4; ++i) q[i] = __builtin_amdgcn_raw_buffer_load_b128(rs, qoff + i * 1024, 0, 16);
        if (wnone(bad16(q[0]) | bad16(q[1]) | bad16(q[2]) | bad16(q[3])) || ++spins > SPIN_CAP) break;
        __builtin_amdgcn_s_sleep(1);
      }
#pragma unroll
      for (int i = 0; i < 4; ++i) lds_st<v4u>(L_Q + (w * 4 + i) * 1024 + lane * 16, q[i]);
    }
    BAR();
    STAMP(2);
    {
      v4u qf[8][2];
#pragma unroll
      for (int ss = 0; ss < 8; ++ss)
#pragma unroll
        for (int ht = 0; ht < 2; ++ht) qf[ss][ht] = lds_ld<v4u>(L_Q + ch * 16384 + ss * 2048 + ht * 1024 + lane * 16);
      v4u cfv = sent4;
      if (w == 7) cfv = ld16s(rs, WS_CF2 + (lane & 31) * 16);
      f32x4 acc[2][2] = {{zero4, zero4}, {zero4, zero4}};
      v2u vt[4][4][2];
      const int q = l15 >> 2, pp = lane & 3;
      unsigned vaddr[2];
#pragma unroll
      for (int t2 = 0; t2 < 2; ++t2) {
        const int row = 8 * g + 4 * t2 + q;
        vaddr[t2] = L_KV + row * 1024 + 8 * (pp & 1);
      }
#pragma unroll
      for (int ss = 0; ss < 8; ++ss) {
#pragma unroll
        for (int kt = 0; kt < 2; ++kt) {
          acc[kt][0] = mfma32(kf[ss][kt], qf[ss][0], acc[kt][0]);
          acc[kt][1] = mfma32(kf[ss][kt], qf[ss][1], acc[kt][1]);
        }
      }
      BAR();
#pragma unroll
      for (int ht = 0; ht < 2; ++ht) lds_st<f32x4>(L_EX + (w * 2 + ht) * 1024 + lane * 16, sel4(ch != 0, acc[0][ht], acc[1][ht]));
#pragma unroll
      for (int ks = 0; ks < 4; ++ks)
#pragma unroll
        for (int j = 0; j < 4; ++j)
#pragma unroll
          for (int t2 = 0; t2 < 2; ++t2) {
            const int rl = (8 * g + 4 * t2 + q) & 15;
            const unsigned addr = vaddr[t2] + (((8 * w + j + 4 * (pp >> 1)) ^ rl) * 16) + (ks >> 1) * 65536;
            asm volatile("ds_read_b64_tr_b16 %0, %1 offset:%2" : "=v"(vt[ks][j][t2]) : "v"(addr), "i"((ks & 1) * 32768) : "memory");
          }
      BAR();
#pragma unroll
      for (int ks = 0; ks < 4; ++ks)
        asm volatile("" : "+v"(vt[ks][0][0]), "+v"(vt[ks][0][1]), "+v"(vt[ks][1][0]), "+v"(vt[ks][1][1]), "+v"(vt[ks][2][0]), "+v"(vt[ks][2][1]), "+v"(vt[ks][3][0]), "+v"(vt[ks][3][1]));
      f32x4 sc[2];
#pragma unroll
      for (int ht = 0; ht < 2; ++ht) sc[ht] = sel4(ch != 0, acc[1][ht], acc[0][ht]) + lds_ld<f32x4>(L_EX + ((w ^ 1) * 2 + ht) * 1024 + lane * 16);
#pragma unroll
      for (int ht = 0; ht < 2; ++ht) {
        float m = fmaxf(fmaxf(sc[ht][0], sc[ht][1]), fmaxf(sc[ht][2], sc[ht][3]));
        m = x32_max(x16_max(m));
        if (lane < 16) lds_st<float>(L_MX + ((ht * 16 + lane) * 8 + w) * 4, m);
      }
      BAR();
#pragma unroll
      for (int ht = 0; ht < 2; ++ht) {
        const f32x4 a = lds_ld<f32x4>(L_MX + (ht * 16 + l15) * 32), c = lds_ld<f32x4>(L_MX + (ht * 16 + l15) * 32 + 16);
        const float mx = fmaxf(fmaxf(fmaxf(a[0], a[1]), fmaxf(a[2], a[3])), fmaxf(fmaxf(c[0], c[1]), fmaxf(c[2], c[3])));
        float pr[4];
#pragma unroll
        for (int e = 0; e < 4; ++e) pr[e] = __builtin_amdgcn_exp2f(sc[ht][e] - mx);
        const v2u pk = {pk2(pr[0], pr[1]), pk2(pr[2], pr[3])};
        float sm = (bflo(pk.x) + bfhi(pk.x)) + (bflo(pk.y) + bfhi(pk.y));
        sm = x32_sum(x16_sum(sm));
        if (lane < 16) lds_st<float>(L_SM + ((ht * 16 + lane) * 8 + w) * 4, sm);
        lds_st<v2u>(L_P + ((ht * 4 + (w >> 1)) * 64 + (2 * (w & 1) + (g >> 1)) * 16 + l15) * 16 + 8 * (g & 1), pk);
      }
      if (w == 7) {
        unsigned spins = 0;
        while (!wnone((cfv.x != tag) | (cfv.y != tag) | (cfv.z != tag) | (cfv.w != tag)) && ++spins <= SPIN_CAP) {
          __builtin_amdgcn_s_sleep(2);
          FENCE();
          cfv = ld16s(rs, WS_CF2 + (lane & 31) * 16);
        }
      }
      BAR();
      STAMP(3);
      if (w == 0 && lane < 32) {
        const f32x4 a = lds_ld<f32x4>(L_SM + lane * 32), c = lds_ld<f32x4>(L_SM + lane * 32 + 16);
        const float l = ((a[0] + a[1]) + (a[2] + a[3])) + ((c[0] + c[1]) + (c[2] + c[3]));
        const f32x4 a2 = lds_ld<f32x4>(L_MX + lane * 32), c2 = lds_ld<f32x4>(L_MX + lane * 32 + 16);
        const float m = fmaxf(fmaxf(fmaxf(a2[0], a2[1]), fmaxf(a2[2], a2[3])), fmaxf(fmaxf(c2[0], c2[1]), fmaxf(c2[2], c2[3])));
        st8s(rs, WS_ST + ((lane * 4 + b) * 32 + s) * 8, v2u{nosent(__float_as_uint(m)), nosent(__float_as_uint(l))});
      }
      v4u pf[4][2];
#pragma unroll
      for (int ks = 0; ks < 4; ++ks)
#pragma unroll
        for (int ht = 0; ht < 2; ++ht) pf[ks][ht] = lds_ld<v4u>(L_P + ((ht * 4 + ks) * 64 + lane) * 16);
      f32x4 oc[4][2];
#pragma unroll
      for (int j = 0; j < 4; ++j) { oc[j][0] = zero4; oc[j][1] = zero4; }
#pragma unroll
      for (int ks = 0; ks < 4; ++ks)
#pragma unroll
        for (int j = 0; j < 4; ++j) {
          const v4u a = {vt[ks][j][0].x, vt[ks][j][0].y, vt[ks][j][1].x, vt[ks][j][1].y};
          oc[j][0] = mfma32(a, pf[ks][0], oc[j][0]);
          oc[j][1] = mfma32(a, pf[ks][1], oc[j][1]);
        }
      const int ooff = WS_OB + ((l15 * 4 + (w >> 1)) * 128 + b * 32 + s) * 256 + ((w & 1) * 8 + g * 2) * 16;
#pragma unroll
      for (int ht = 0; ht < 2; ++ht)
#pragma unroll
        for (int jp = 0; jp < 2; ++jp) {
          const v4u o = {nosent(pk2(oc[2 * jp][ht][0], oc[2 * jp][ht][1])), nosent(pk2(oc[2 * jp][ht][2], oc[2 * jp][ht][3])),
                         nosent(pk2(oc[2 * jp + 1][ht][0], oc[2 * jp + 1][ht][1])), nosent(pk2(oc[2 * jp + 1][ht][2], oc[2 * jp + 1][ht][3]))};
          __builtin_amdgcn_raw_buffer_store_b128(o, rs, ooff + jp * 16, ht * (16 * 4 * 128 * 256), 16);
        }
    }
    STAMP(4);
    if (!isG) { FLUSH_STAMPS(); return; }
    constexpr unsigned L_GA = 0;
    constexpr unsigned L_RG = 21504;
    constexpr unsigned L_LN = 38912;
    constexpr unsigned L_HH = 39168;
    v4u fg[3][4];
    {
      v2u vg[3][8];
#pragma unroll
      for (int n = 0; n < 3; ++n) {
        const int kb = min(w + 8 * n, 19);
        const u16* src = ((l15 >> 3) ? p.wu : p.wg) + (size_t)(32 * kb + 8 * g) * DF + 32 * gp + 4 * (l15 & 7);
#pragma unroll
        for (int i = 0; i < 8; ++i) vg[n][i] = ldg<v2u>(src + (size_t)i * DF);
      }
#pragma unroll
      for (int n = 0; n < 3; ++n) tr8x4(vg[n], fg[n]);
    }
    v4u fd[2][4];
    {
      const rsrc_t rs_wd = mkrs(p.wd, DF * DM * 2);
      v2u vd[2][8];
#pragma unroll
      for (int n = 0; n < 2; ++n) {
        const int u = min(w + 8 * n, 9);
        const int off = ((32 * gp + 8 * g) * DM + 64 * u + 4 * l15) * 2;
#pragma unroll
        for (int i = 0; i < 8; ++i) vd[n][i] = bl8(rs_wd, off, i * (DM * 2));
      }
#pragma unroll
      for (int n = 0; n < 2; ++n) tr8x4(vd[n], fd[n]);
    }
    BAR();
    const int gb_ = w >> 1;
    const rsrc_t rs_x = mkrs(p.x, NB * DM * 2);
    f32x4 ga4[2], cb4[2];
    v2u x4[2];
    int m0[2];
    m0[0] = 4 * (80 * (w & 1) + lane);
    m0[1] = 4 * (80 * (w & 1) + 64 + (lane & 15));
    {
      v4u a[2], c2[2], s2[2];
      unsigned spins = 0;
#pragma unroll
      for (int n = 0; n < 2; ++n) x4[n] = bl8(rs_x, (gb_ * DM + m0[n]) * 2, 0);
      for (;;) {
        FENCE();
        unsigned bad = 0;
#pragma unroll
        for (int n = 0; n < 2; ++n) {
          a[n] = ld16s(rs, WS_MP + (0 * 4 + gb_) * 2560 + m0[n] * 4);
          s2[n] = ld16s(rs, WS_MP + (2 * 4 + gb_) * 2560 + m0[n] * 4);
          c2[n] = ld16s(rs, WS_MP + (3 * 4 + gb_) * 2560 + m0[n] * 4);
        }
#pragma unroll
        for (int n = 0; n < 2; ++n) bad |= bad16(a[n]) | bad16(s2[n]) | bad16(c2[n]);
        if (wnone(bad) || ++spins > SPIN_CAP) break;
        __builtin_amdgcn_s_sleep(4);
      }
#pragma unroll
      for (int n = 0; n < 2; ++n) {
        ga4[n] = __builtin_bit_cast(f32x4, a[n]);
        cb4[n] = __builtin_bit_cast(f32x4, c2[n]);
        const f32x4 sb = __builtin_bit_cast(f32x4, s2[n]);
        lds_st<v2u>(L_GA + (4 * gb_ + 2) * 1296 + m0[n] * 2, v2u{pk2(cb4[n][0], cb4[n][1]), pk2(cb4[n][2], cb4[n][3])});
        lds_st<v2u>(L_GA + (4 * gb_ + 3) * 1296 + m0[n] * 2, v2u{pk2(sb[0], sb[1]), pk2(sb[2], sb[3])});
      }
    }
    BAR();
    {
      f32x4 ac[4] = {zero4, zero4, zero4, zero4};
#pragma unroll
      for (int n = 0; n < 3; ++n) {
        const int kb = w + 8 * n;
        if (kb < 20) {
          const v4u a = lds_ld<v4u>(L_GA + l15 * 1296 + (32 * kb + 8 * g) * 2);
#pragma unroll
          for (int j = 0; j < 4; ++j) ac[j] = mfma32(a, fg[n][j], ac[j]);
        }
      }
      lds_st<f32x4>(L_RG + (((w * 2 + 0) * 4 + g) * 64 + 4 * l15) * 4, f32x4{ac[0][2], ac[1][2], ac[2][2], ac[3][2]});
      lds_st<f32x4>(L_RG + (((w * 2 + 1) * 4 + g) * 64 + 4 * l15) * 4, f32x4{ac[0][3], ac[1][3], ac[2][3], ac[3][3]});
    }
    BAR();
    float U1 = 0.f, U2 = 0.f;
    if (w < 4) {
#pragma unroll
      for (int ww = 0; ww < 8; ++ww) {
        U1 += lds_ld<float>(L_RG + (((ww * 2 + 0) * 4 + w) * 64 + lane) * 4);
        U2 += lds_ld<float>(L_RG + (((ww * 2 + 1) * 4 + w) * 64 + lane) * 4);
      }
    }
    BAR();
    STAMP(5);
    {
      float s1 = 0.f, s2 = 0.f;
      v4u a[2][4];
      unsigned spins = 0;
      for (;;) {
        FENCE();
        unsigned bad = 0;
#pragma unroll
        for (int n = 0; n < 2; ++n)
#pragma unroll
          for (int kr = 0; kr < 4; ++kr) a[n][kr] = __builtin_amdgcn_raw_buffer_load_b128(rs, WS_PO + gb_ * 2560 + m0[n] * 4, kr * 10240, 16);
#pragma unroll
        for (int n = 0; n < 2; ++n)
#pragma unroll
          for (int kr = 0; kr < 4; ++kr) bad |= bad16(a[n][kr]);
        if (wnone(bad) || ++spins > SPIN_CAP) break;
        __builtin_amdgcn_s_sleep(1);
      }
      STAMP(6);
#pragma unroll
      for (int n = 0; n < 2; ++n) {
        const f32x4 ao = (__builtin_bit_cast(f32x4, a[n][0]) + __builtin_bit_cast(f32x4, a[n][1])) + (__builtin_bit_cast(f32x4, a[n][2]) + __builtin_bit_cast(f32x4, a[n][3]));
        const float xv[4] = {bflo(x4[n].x), bfhi(x4[n].x), bflo(x4[n].y), bfhi(x4[n].y)};
        float y[4];
        const float live = (n == 0 || lane < 16) ? 1.f : 0.f;
#pragma unroll
        for (int e = 0; e < 4; ++e) {
          const float x2 = xv[e] + ga4[n][e] * ao[e];
          s1 += live * x2; s2 += live * x2 * x2;
          y[e] = x2 * cb4[n][e];
        }
        const unsigned h01 = pk2(y[0], y[1]), h23 = pk2(y[2], y[3]);
        const unsigned l01 = pk2(y[0] - bflo(h01), y[1] - bfhi(h01)), l23 = pk2(y[2] - bflo(h23), y[3] - bfhi(h23));
        lds_st<v2u>(L_GA + (4 * gb_ + 0) * 1296 + m0[n] * 2, v2u{h01, h23});
        lds_st<v2u>(L_GA + (4 * gb_ + 1) * 1296 + m0[n] * 2, v2u{l01, l23});
      }
      s1 = x32_sum(x16_sum(sum16(s1)));
      s2 = x32_sum(x16_sum(sum16(s2)));
      if (lane == 0) lds_st<v2u>(L_LN + w * 8, v2u{__float_as_uint(s1), __float_as_uint(s2)});
    }
    BAR();
    {
      f32x4 ac[4] = {zero4, zero4, zero4, zero4};
#pragma unroll
      for (int n = 0; n < 3; ++n) {
        const int kb = w + 8 * n;
        if (kb < 20) {
          const v4u a = lds_ld<v4u>(L_GA + l15 * 1296 + (32 * kb + 8 * g) * 2);
#pragma unroll
          for (int j = 0; j < 4; ++j) ac[j] = mfma32(a, fg[n][j], ac[j]);
        }
      }
      lds_st<f32x4>(L_RG + (((w * 2 + 0) * 4 + g) * 64 + 4 * l15) * 4, f32x4{ac[0][0] + ac[0][1], ac[1][0] + ac[1][1], ac[2][0] + ac[2][1], ac[3][0] + ac[3][1]});
    }
    BAR();
    if (w < 4) {
      float T = 0.f;
#pragma unroll
      for (int ww = 0; ww < 8; ++ww) T += lds_ld<float>(L_RG + (((ww * 2 + 0) * 4 + w) * 64 + lane) * 4);
      const v4u ln = lds_ld<v4u>(L_LN + w * 16);
      const float s1 = __uint_as_float(ln.x) + __uint_as_float(ln.z), s2 = __uint_as_float(ln.y) + __uint_as_float(ln.w);
      const float mean = s1 * (1.f / DM);
      const float rstd = rsqrtf(fmaxf(s2 * (1.f / DM) - mean * mean, 0.f) + 1e-5f);
      const float val = rstd * (T - mean * U1) + U2;
      auto sw = __builtin_amdgcn_permlane32_swap(__float_as_uint(val), __float_as_uint(val), false, false);
      const float gv = __uint_as_float(sw[0]), uv = __uint_as_float(sw[1]);
      const float z = 0.7978845608028654f * (gv + 0.044715f * gv * gv * gv);
      const float th = 1.f - 2.f * __builtin_amdgcn_rcpf(1.f + __expf(2.f * z));
      const float hh = 0.5f * gv * (1.f + th) * uv;
      const unsigned hi = pk2(hh, 0.f) & 0xFFFFu;
      const unsigned lo = pk2(hh - bf2f(hi), 0.f) & 0xFFFFu;
      lds_st<u16>(L_HH + (4 * w + (lane >> 5)) * 80 + (lane & 31) * 2, (u16)(lane < 32 ? hi : lo));
    }
    BAR();
    {
      const v4u a = lds_ld<v4u>(L_HH + l15 * 80 + g * 16);
#pragma unroll
      for (int n = 0; n < 2; ++n) {
        const int u = w + 8 * n;
        if (u < 10) {
          f32x4 o;
#pragma unroll
          for (int j = 0; j < 4; ++j) { const f32x4 d = mfma32(a, fd[n][j], zero4); o[j] = d[0] + d[1]; }
          const v4u ov = {nosent(__float_as_uint(o[0])), nosent(__float_as_uint(o[1])), nosent(__float_as_uint(o[2])), nosent(__float_as_uint(o[3]))};
          st16s(rs, WS_PD + (8 * u + (l15 >> 1)) * 6912 + gp * 128 + g * 32 + (l15 & 1) * 16, ov);
        }
      }
    }
    STAMP(7);
    FLUSH_STAMPS();
  }
}

extern "C" void kernel_launch(void* const* d_in, const int* in_sizes, int n_in, void* d_out, int out_size, void* d_ws, size_t ws_size, hipStream_t stream) {
  Params p;
  p.x = (const u16*)d_in[0]; p.ckv = (const u16*)d_in[1]; p.wq = (const u16*)d_in[2]; p.wuk = (const u16*)d_in[3];
  p.wuv = (const u16*)d_in[4]; p.wo = (const u16*)d_in[5]; p.wg = (const u16*)d_in[7]; p.wu = (const u16*)d_in[8];
  p.wd = (const u16*)d_in[9]; p.c = (const u16*)d_in[10]; p.wmod = (const u16*)d_in[11];
  p.out = (u16*)d_out; p.ws = (unsigned char*)d_ws;
  (void)hipFuncSetAttribute((const void*)mega, hipFuncAttributeMaxDynamicSharedMemorySize, LDS_BYTES);
  hipLaunchKernelGGL(mega, dim3(256), dim3(512), LDS_BYTES, stream, p);
}
```

```cpp
#include <hip/hip_runtime.h>
#include <stdint.h>

typedef unsigned short u16;
typedef unsigned v4u __attribute__((ext_vector_type(4)));
typedef unsigned v2u __attribute__((ext_vector_type(2)));
typedef float f32x4 __attribute__((ext_vector_type(4)));
typedef __bf16 bf16x8 __attribute__((ext_vector_type(8)));
typedef short s16x4 __attribute__((ext_vector_type(4)));
#define GAS __attribute__((address_space(1)))
#define LAS __attribute__((address_space(3)))
#define DEV __device__ __forceinline__

#define ROT 7
#define H1_T 260
#define O_DELAY 40
#define KV_DELAY 0
#define POLL_SLEEP 2
#define STAMPS 0

constexpr int NB = 4, NH = 32, SK = 4096, HD = 128, DM = 640, DC = 512, DF = 1728;
constexpr unsigned SENT = 0xFFFFFFFFu;
constexpr unsigned SPIN_CAP = 1u << 16;

constexpr int WS_EPOCH = 0;
constexpr int WS_CF1 = 0x180000 + 0x40000;
constexpr int WS_CF2 = 0x180000 + 0x50000;
constexpr int WS_HB = 0x100000;
constexpr int WS_QP = 0x10000;
constexpr int WS_QB = 0x30000;
constexpr int WS_ST = 0x50000;
constexpr int WS_PX = 0x60000;
constexpr int WS_PO = 0x80000;
constexpr int WS_MP = 0x90000;
constexpr int WS_PD = 0x120000;
constexpr int WS_OB = 0x200000;
constexpr int WS_STAMP = 0x800000;
constexpr int WS_BYTES = 0x1000000;
constexpr int LDS_BYTES = 163840;

struct Params {
  const u16 *x, *ckv, *wq, *wuk, *wuv, *wo, *wg, *wu, *wd, *c, *wmod;
  u16* out;
  unsigned char* ws;
};

DEV float bf2f(unsigned b) { return __uint_as_float(b << 16); }
DEV float bflo(unsigned w) { return __uint_as_float(w << 16); }
DEV float bfhi(unsigned w) { return __uint_as_float(w & 0xFFFF0000u); }
DEV unsigned pk2(float lo, float hi) {
  typedef __bf16 bf2 __attribute__((ext_vector_type(2)));
  typedef float f2 __attribute__((ext_vector_type(2)));
  f2 v = {lo, hi};
  bf2 r = __builtin_convertvector(v, bf2);
  return __builtin_bit_cast(unsigned, r);
}
DEV unsigned nosent(unsigned v) { return v < 0xFFFFFFFEu ? v : 0xFFFFFFFEu; }
DEV f32x4 mfma32(v4u a, v4u b, f32x4 c) {
  return __builtin_amdgcn_mfma_f32_16x16x32_bf16(__builtin_bit_cast(bf16x8, a), __builtin_bit_cast(bf16x8, b), c, 0, 0, 0);
}
DEV f32x4 mfma16(v2u a, v2u b, f32x4 c) {
  return __builtin_amdgcn_mfma_f32_16x16x16bf16_1k(__builtin_bit_cast(s16x4, a), __builtin_bit_cast(s16x4, b), c, 0, 0, 0);
}
constexpr unsigned PLO = 0x05040100u, PHI = 0x07060302u;
DEV unsigned plo(unsigned hi_row, unsigned lo_row) { return __builtin_amdgcn_perm(hi_row, lo_row, PLO); }
DEV unsigned phi(unsigned hi_row, unsigned lo_row) { return __builtin_amdgcn_perm(hi_row, lo_row, PHI); }
DEV void tr8x8(const v4u (&v)[8], v4u (&f)[8]) {
#pragma unroll
  for (int q = 0; q < 4; ++q) {
#pragma unroll
    for (int t = 0; t < 4; ++t) {
      f[2 * q][t] = plo(v[2 * t + 1][q], v[2 * t][q]);
      f[2 * q + 1][t] = phi(v[2 * t + 1][q], v[2 * t][q]);
    }
  }
}
DEV void tr8x4(const v2u (&v)[8], v4u (&f)[4]) {
#pragma unroll
  for (int q = 0; q < 2; ++q) {
#pragma unroll
    for (int t = 0; t < 4; ++t) {
      f[2 * q][t] = plo(v[2 * t + 1][q], v[2 * t][q]);
      f[2 * q + 1][t] = phi(v[2 * t + 1][q], v[2 * t][q]);
    }
  }
}
DEV void tr8x2(const unsigned (&v)[8], v4u (&f)[2]) {
#pragma unroll
  for (int t = 0; t < 4; ++t) {
    f[0][t] = plo(v[2 * t + 1], v[2 * t]);
    f[1][t] = phi(v[2 * t + 1], v[2 * t]);
  }
}
DEV unsigned tagf(unsigned e) {
  e += 0x9E3779B9u; e ^= e >> 16; e *= 0x85EBCA6Bu; e ^= e >> 13; e *= 0xC2B2AE35u; e ^= e >> 16; return e;
}
#define BAR() asm volatile("s_waitcnt lgkmcnt(0)\n\ts_barrier" ::: "memory")
#define VMWAIT(n) asm volatile("s_waitcnt vmcnt(" #n ")" ::: "memory")
#define FENCE() asm volatile("" ::: "memory")
typedef __amdgpu_buffer_rsrc_t rsrc_t;
DEV v4u ld16s(rsrc_t rs, int off) { return __builtin_amdgcn_raw_buffer_load_b128(rs, off, 0, 16); }
DEV v2u ld8s(rsrc_t rs, int off) { return __builtin_amdgcn_raw_buffer_load_b64(rs, off, 0, 16); }
DEV unsigned ld4s(rsrc_t rs, int off) { return __builtin_amdgcn_raw_buffer_load_b32(rs, off, 0, 16); }
DEV void st16s(rsrc_t rs, int off, v4u v) { __builtin_amdgcn_raw_buffer_store_b128(v, rs, off, 0, 16); }
DEV void st8s(rsrc_t rs, int off, v2u v) { __builtin_amdgcn_raw_buffer_store_b64(v, rs, off, 0, 16); }
DEV void st4s(rsrc_t rs, int off, unsigned v) { __builtin_amdgcn_raw_buffer_store_b32(v, rs, off, 0, 16); }
DEV bool ok16(v4u v) {
  unsigned m = max(max(v.x, v.y), max(v.z, v.w));
  return m != SENT;
}
DEV bool ok8(v2u v) { return max(v.x, v.y) != SENT; }
template <typename T> DEV T ldg(const void* p) { return *(const GAS T*)p; }
template <typename T> DEV T ldnt(const void* p) { return __builtin_nontemporal_load((const GAS T*)p); }
template <typename T> DEV T lds_ld(unsigned off) { return *(const LAS T*)(uintptr_t)off; }
template <typename T> DEV void lds_st(unsigned off, T v) { *(LAS T*)(uintptr_t)off = v; }

DEV float dpp_xor1(float v) { return __builtin_bit_cast(float, __builtin_amdgcn_mov_dpp(__builtin_bit_cast(int, v), 0xB1, 0xf, 0xf, true)); }
DEV float dpp_xor2(float v) { return __builtin_bit_cast(float, __builtin_amdgcn_mov_dpp(__builtin_bit_cast(int, v), 0x4E, 0xf, 0xf, true)); }
DEV float dpp_ror4(float v) { return __builtin_bit_cast(float, __builtin_amdgcn_mov_dpp(__builtin_bit_cast(int, v), 0x124, 0xf, 0xf, true)); }
DEV float dpp_ror8(float v) { return __builtin_bit_cast(float, __builtin_amdgcn_mov_dpp(__builtin_bit_cast(int, v), 0x128, 0xf, 0xf, true)); }
DEV float sum16(float v) { v += dpp_xor1(v); v += dpp_xor2(v); v += dpp_ror4(v); v += dpp_ror8(v); return v; }
DEV float x16_sum(float x) {
  auto s = __builtin_amdgcn_permlane16_swap(__float_as_uint(x), __float_as_uint(x), false, false);
  return __uint_as_float(s[0]) + __uint_as_float(s[1]);
}
DEV float x32_sum(float x) {
  auto t = __builtin_amdgcn_permlane32_swap(__float_as_uint(x), __float_as_uint(x), false, false);
  return __uint_as_float(t[0]) + __uint_as_float(t[1]);
}
DEV float x16_max(float x) {
  auto s = __builtin_amdgcn_permlane16_swap(__float_as_uint(x), __float_as_uint(x), false, false);
  return fmaxf(__uint_as_float(s[0]), __uint_as_float(s[1]));
}
DEV float x32_max(float x) {
  auto t = __builtin_amdgcn_permlane32_swap(__float_as_uint(x), __float_as_uint(x), false, false);
  return fmaxf(__uint_as_float(t[0]), __uint_as_float(t[1]));
}
DEV float max16(float v) { v = fmaxf(v, dpp_xor1(v)); v = fmaxf(v, dpp_xor2(v)); v = fmaxf(v, dpp_ror4(v)); v = fmaxf(v, dpp_ror8(v)); return v; }
DEV f32x4 sel4(bool c, f32x4 a, f32x4 b) { f32x4 r; r[0] = c ? a[0] : b[0]; r[1] = c ? a[1] : b[1]; r[2] = c ? a[2] : b[2]; r[3] = c ? a[3] : b[3]; return r; }
DEV v2u sel2(bool c, v2u a, v2u b) { v2u r; r[0] = c ? a[0] : b[0]; r[1] = c ? a[1] : b[1]; return r; }
DEV bool wall(bool p) { return __builtin_amdgcn_ballot_w64(!p) == 0ull; }

#define STAMP(i) do { } while (0)
#define FLUSH_STAMPS() do { } while (0)
DEV rsrc_t mkrs(const void* ptr, int bytes) { return __builtin_amdgcn_make_buffer_rsrc((void*)ptr, 0, bytes, 0x00020000); }
DEV v4u bl16(rsrc_t r, int voff, int soff) { return __builtin_amdgcn_raw_buffer_load_b128(r, voff, soff, 0); }
DEV v4u bl16nt(rsrc_t r, int voff, int soff) { return __builtin_amdgcn_raw_buffer_load_b128(r, voff, soff, 2); }
DEV v2u bl8(rsrc_t r, int voff, int soff) { return __builtin_amdgcn_raw_buffer_load_b64(r, voff, soff, 0); }
DEV unsigned bl4(rsrc_t r, int voff, int soff) { return __builtin_amdgcn_raw_buffer_load_b32(r, voff, soff, 0); }
DEV unsigned bad16(v4u v) { return (unsigned)(max(max(v.x, v.y), max(v.z, v.w)) == SENT); }
DEV bool wnone(unsigned bad) { return __builtin_amdgcn_ballot_w64(bad != 0u) == 0ull; }
DEV float silu(float v) { return v * __builtin_amdgcn_rcpf(1.f + __expf(-v)); }
DEV v4u silu_frag(v4u cv, bool lo_lane) {
  v4u o;
#pragma unroll
  for (int q = 0; q < 4; ++q) {
    const float a = silu(bflo(cv[q])), b = silu(bfhi(cv[q]));
    const unsigned hi = pk2(a, b);
    const unsigned lo = pk2(a - bflo(hi), b - bfhi(hi));
    o[q] = lo_lane ? lo : hi;
  }
  return o;
}

template <int NL> DEV void wait_flags(rsrc_t rs, int off, unsigned tag) {
  unsigned spins = 0;
  for (;;) {
    FENCE();
    unsigned bad = 0;
    unsigned v[NL];
#pragma unroll
    for (int i = 0; i < NL; ++i) v[i] = __builtin_amdgcn_raw_buffer_load_b32(rs, off, i * 8192, 16);
#pragma unroll
    for (int i = 0; i < NL; ++i) bad |= v[i] ^ tag;
    if (wnone(bad) || ++spins > SPIN_CAP) break;
    __builtin_amdgcn_s_sleep(6);
  }
}
struct FW { v4u f[4][2]; };
DEV void f_load(const Params& p, int ft, int w, int l15, int g, unsigned (&vf)[4][8]) {
  const rsrc_t rs_wo = mkrs(p.wo, NH * HD * DM * 2);
  const int kr = ft / 20, ms = ft % 20;
  const int off = ((1024 * kr + 128 * w + 8 * g) * DM + 32 * ms + 2 * l15) * 2;
#pragma unroll
  for (int ks = 0; ks < 4; ++ks)
#pragma unroll
    for (int i = 0; i < 8; ++i) vf[ks][i] = bl4(rs_wo, off, (32 * ks + i) * (DM * 2));
}
DEV void f_run(rsrc_t rs, int ft, int tid, int w, int l15, int g, const v4u (&ff)[4][2], unsigned l_rf) {
  const f32x4 zero4 = {0.f, 0.f, 0.f, 0.f};
  const int kr = ft / 20, ms = ft % 20;
  v4u pa[4];
  unsigned spins = 0;
  const int poff = WS_PX + (((8 * kr + w) * 4 + (l15 & 3)) * 4 + (l15 >> 2)) * 256 + g * 16;
  for (;;) {
    FENCE();
#pragma unroll
    for (int ks = 0; ks < 4; ++ks) pa[ks] = __builtin_amdgcn_raw_buffer_load_b128(rs, poff + ks * 64, 0, 16);
    if (wnone(bad16(pa[0]) | bad16(pa[1]) | bad16(pa[2]) | bad16(pa[3])) || ++spins > SPIN_CAP) break;
    __builtin_amdgcn_s_sleep(POLL_SLEEP);
  }
  f32x4 d0 = zero4, d1 = zero4;
#pragma unroll
  for (int ks = 0; ks < 4; ++ks) { d0 = mfma32(pa[ks], ff[ks][0], d0); d1 = mfma32(pa[ks], ff[ks][1], d1); }
  const float o0 = (d0[0] + d0[1]) + (d0[2] + d0[3]), o1 = (d1[0] + d1[1]) + (d1[2] + d1[3]);
  lds_st<v2u>(l_rf + ((w * 4 + g) * 32 + 2 * l15) * 4, v2u{__float_as_uint(o0), __float_as_uint(o1)});
  BAR();
  if (w < 2) {
    const int b = tid >> 5, cl = tid & 31;
    float s = 0.f;
#pragma unroll
    for (int ww = 0; ww < 8; ++ww) s += lds_ld<float>(l_rf + ((ww * 4 + b) * 32 + cl) * 4);
    st4s(rs, WS_PO + (kr * 4 + b) * 2560 + (32 * ms + cl) * 4, nosent(__float_as_uint(s)));
  }
}
DEV void f_clear(rsrc_t rs, int ft, int lane) {
  const v4u sent4 = {SENT, SENT, SENT, SENT};
  if (lane < 32) st16s(rs, WS_PO + ((ft / 20) * 4 + (lane >> 3)) * 2560 + (ft % 20) * 128 + (lane & 7) * 16, sent4);
}

__global__ void __launch_bounds__(512, 2) mega(Params p) {
  extern __shared__ __attribute__((aligned(16))) unsigned char smem_[];
  STAMP(0);
  const unsigned long long t_start = __builtin_amdgcn_s_memrealtime();
  const int tid = threadIdx.x, lane = tid & 63;
  const int w = __builtin_amdgcn_readfirstlane(tid >> 6);
  const int x8 = blockIdx.x & 7, slot = blockIdx.x >> 3;
  const int xg = (x8 + ROT) & 7;
  const rsrc_t rs = mkrs(p.ws, WS_BYTES);
  const int l15 = lane & 15, g = lane >> 4;
  const unsigned epoch = *(const __attribute__((address_space(4))) unsigned*)(p.ws + WS_EPOCH);
  const unsigned tag = tagf(epoch);
  const v4u sent4 = {SENT, SENT, SENT, SENT};
  const f32x4 zero4 = {0.f, 0.f, 0.f, 0.f};

  if (xg < 4) {
    const int h = xg * 8 + (slot >> 2), q4 = slot & 3;
    const bool isA = slot < 20;
    const int r = xg * 20 + slot;
    const int ft = xg * 12 + (slot - 20);
    constexpr unsigned L_RA = 0;
    constexpr unsigned L_HA = 8192;
    constexpr unsigned L_RB = 16384;
    constexpr unsigned L_QS = 27648;
    constexpr unsigned L_W = 28672;
    constexpr unsigned L_CC = 30720;
    constexpr unsigned L_RF = 36864;
    constexpr unsigned L_R2 = 40960;
    const rsrc_t rs_x = mkrs(p.x, NB * DM * 2);
    const rsrc_t rs_c = mkrs(p.c, NB * DM * 2);
    const rsrc_t rs_wmod = mkrs(p.wmod, DM * 6 * DM * 2);

    v4u fq[8];
    v4u vk[4];
    const rsrc_t rs_wuk = mkrs(p.wuk, DC * NH * HD * 2);
    const int koff = (((128 * q4 + 16 * w + l15) * NH + h) * HD + 8 * g) * 2;
    if (w < 5) {
      const rsrc_t rs_wq = mkrs(p.wq, DM * NH * HD * 2);
      const int qoff = ((160 * q4 + 32 * w + 8 * g) * (NH * HD) + HD * h + 8 * l15) * 2;
      v4u vq[8];
      if (isA) {
        const v4u cf = bl16(rs_c, ((l15 >> 2) * DM + 160 * xg + 32 * w + 8 * g) * 2, 0);
        v2u va[8];
        const int aoff = ((160 * xg + 32 * w + 8 * g) * (6 * DM) + 64 * slot + 4 * l15) * 2;
#pragma unroll
        for (int i = 0; i < 8; ++i) va[i] = __builtin_amdgcn_raw_buffer_load_b64(rs_wmod, aoff, i * (6 * DM * 2), 2);
        __builtin_amdgcn_sched_barrier(0);
        __builtin_amdgcn_s_barrier();
        __builtin_amdgcn_sched_barrier(0);
#pragma unroll
        for (int i = 0; i < 8; ++i) vq[i] = bl16nt(rs_wq, qoff, i * (NH * HD * 2));
        __builtin_amdgcn_sched_barrier(0);
#pragma unroll
        for (int ks = 0; ks < 4; ++ks) vk[ks] = bl16nt(rs_wuk, koff, ks * 64);
        __builtin_amdgcn_sched_barrier(0);
        const v4u af = silu_frag(cf, lane & 1);
        v4u fa[4];
        tr8x4(va, fa);
        f32x4 o;
#pragma unroll
        for (int j = 0; j < 4; ++j) { const f32x4 d = mfma32(af, fa[j], zero4); o[j] = d[0] + d[1]; }
        lds_st<f32x4>(L_RA + ((w * 4 + g) * 64 + 4 * l15) * 4, o);
        STAMP(1);
        BAR();
        if (w < 2) {
          const int b = tid >> 5, pr = tid & 31;
          float s0 = 0.f, s1 = 0.f;
#pragma unroll
          for (int ww = 0; ww < 5; ++ww) {
            const v2u t2 = lds_ld<v2u>(L_RA + ((ww * 4 + b) * 64 + 2 * pr) * 4);
            s0 += __uint_as_float(t2.x); s1 += __uint_as_float(t2.y);
          }
          const int part = slot >= 10, cbk = slot - 10 * part;
          st8s(rs, WS_HB + (((part * 4 + xg) * 4 + b) * 320 + 32 * cbk + pr) * 8, v2u{pk2(s0, s1), tag});
        }
        STAMP(2);
      } else {
        __builtin_amdgcn_s_barrier();
        __builtin_amdgcn_sched_barrier(0);
#pragma unroll
        for (int i = 0; i < 8; ++i) vq[i] = bl16nt(rs_wq, qoff, i * (NH * HD * 2));
        __builtin_amdgcn_sched_barrier(0);
#pragma unroll
        for (int ks = 0; ks < 4; ++ks) vk[ks] = bl16nt(rs_wuk, koff, ks * 64);
        __builtin_amdgcn_sched_barrier(0);
      }
      tr8x8(vq, fq);
      asm volatile("" : "+v"(vk[0]), "+v"(vk[1]), "+v"(vk[2]), "+v"(vk[3]));
      STAMP(16);
      if (w == 0) {
        wait_flags<1>(rs, WS_CF1 + (((4 * (slot >> 2) + (lane & 3)) << 3) + x8) * 128, tag);
        const int qpoff = WS_QP + h * 4096 + lane * 16;
        v4u t0 = ld16s(rs, qpoff), t1 = ld16s(rs, qpoff + 1024), t2 = ld16s(rs, qpoff + 2048), t3 = ld16s(rs, qpoff + 3072);
        asm volatile("" :: "v"(t0), "v"(t1), "v"(t2), "v"(t3));
      }
    } else {
      const int v3 = w - 5;
      const int mq = min(16 * v3 + l15, 39);
      if (w == 7) {
        st16s(rs, WS_QP + (h * 4 + q4) * 1024 + lane * 16, sent4);
        st16s(rs, WS_PX + (h * 4 + q4) * 1024 + lane * 16, sent4);
        st16s(rs, WS_QB + (g * 32 + h) * 1024 + q4 * 256 + l15 * 16, sent4);
        if (isA) { if (lane < 32) st16s(rs, WS_MP + (xg * 4 + (lane >> 3)) * 2560 + slot * 128 + (lane & 7) * 16, sent4); }
        else f_clear(rs, ft, lane);
      }
      v4u xs[5];
#pragma unroll
      for (int it = 0; it < 5; ++it) xs[it] = bl16(rs_x, (g * DM + 8 * l15) * 2, it * 256);
      const v2u xq = bl8(rs_x, (g * DM + 160 * q4 + 4 * mq) * 2, 0);
      __builtin_amdgcn_sched_barrier(0);
      __builtin_amdgcn_s_barrier();
      __builtin_amdgcn_sched_barrier(0);
      if (w == 7) {
        VMWAIT(0);
        if (lane == 0) st4s(rs, WS_CF1 + blockIdx.x * 128, tag);
      }
#pragma unroll
      for (int ks = 0; ks < 4; ++ks) vk[ks] = bl16nt(rs_wuk, koff, ks * 64);
      __builtin_amdgcn_sched_barrier(0);
      if (isA) BAR();
      float s1 = 0.f, s2 = 0.f;
#pragma unroll
      for (int it = 0; it < 5; ++it)
#pragma unroll
        for (int q = 0; q < 4; ++q) { const float a = bflo(xs[it][q]), b2 = bfhi(xs[it][q]); s1 += a + b2; s2 += a * a + b2 * b2; }
      s1 = sum16(s1); s2 = sum16(s2);
      const float mean = s1 * (1.f / DM);
      const float rstd = rsqrtf(fmaxf(s2 * (1.f / DM) - mean * mean, 0.f) + 1e-5f);
      float xn[4] = {(bflo(xq.x) - mean) * rstd, (bfhi(xq.x) - mean) * rstd, (bflo(xq.y) - mean) * rstd, (bfhi(xq.y) - mean) * rstd};
      while (__builtin_amdgcn_s_memrealtime() - t_start < H1_T) __builtin_amdgcn_s_sleep(2);
      STAMP(18);
      v4u hv[8];
      const int hoff = WS_HB + (g * 320 + 80 * q4 + 2 * mq) * 8;
      unsigned spins = 0;
      for (;;) {
        FENCE();
        unsigned bad = 0;
#pragma unroll
        for (int i = 0; i < 8; ++i) hv[i] = __builtin_amdgcn_raw_buffer_load_b128(rs, hoff, i * 10240, 16);
#pragma unroll
        for (int i = 0; i < 8; ++i) bad |= (hv[i].y ^ tag) | (hv[i].w ^ tag);
        if (wnone(bad) || ++spins > SPIN_CAP) break;
        __builtin_amdgcn_s_sleep(POLL_SLEEP);
      }
      float hh[4];
#pragma unroll
      for (int e = 0; e < 4; ++e) {
        float sa = 0.f, ca = 1.f;
#pragma unroll
        for (int kq = 0; kq < 4; ++kq) {
          const unsigned ws_ = hv[kq][(e >> 1) * 2], wc_ = hv[4 + kq][(e >> 1) * 2];
          sa += (e & 1) ? bfhi(ws_) : bflo(ws_);
          ca += (e & 1) ? bfhi(wc_) : bflo(wc_);
        }
        hh[e] = xn[e] * ca + sa;
      }
      const unsigned h01 = pk2(hh[0], hh[1]), h23 = pk2(hh[2], hh[3]);
      const unsigned l01 = pk2(hh[0] - bflo(h01), hh[1] - bfhi(h01)), l23 = pk2(hh[2] - bflo(h23), hh[3] - bfhi(h23));
      lds_st<v2u>(L_HA + (4 * g + 0) * 336 + mq * 8, v2u{h01, h23});
      lds_st<v2u>(L_HA + (4 * g + 1) * 336 + mq * 8, v2u{l01, l23});
    }
    STAMP(3);
    BAR();
    STAMP(4);
    if (w < 5) {
      const v4u a = lds_ld<v4u>(L_HA + l15 * 336 + (32 * w + 8 * g) * 2);
      float o[8];
#pragma unroll
      for (int j = 0; j < 8; ++j) { const f32x4 d = mfma32(a, fq[j], zero4); o[j] = d[0] + d[1]; }
      const unsigned off = L_RB + ((w * 4 + g) * 128 + 8 * l15) * 4;
      lds_st<f32x4>(off, f32x4{o[0], o[1], o[2], o[3]});
      lds_st<f32x4>(off + 16, f32x4{o[4], o[5], o[6], o[7]});
    }
    BAR();
    if (w < 4) {
      float s0 = 0.f, s1 = 0.f;
#pragma unroll
      for (int ww = 0; ww < 5; ++ww) {
        const v2u t2 = lds_ld<v2u>(L_RB + ((ww * 4 + w) * 128 + 2 * lane) * 4);
        s0 += __uint_as_float(t2.x); s1 += __uint_as_float(t2.y);
      }
      st4s(rs, WS_QP + ((h * 4 + q4) * 4 + w) * 256 + lane * 4, nosent(pk2(s0, s1)));
    }
    STAMP(5);
    {
      const int qpoff = WS_QP + ((h * 4 + (l15 & 3)) * 4 + (l15 >> 2)) * 256 + g * 16;
      v4u qa[4];
      unsigned spins = 0;
      for (;;) {
        FENCE();
#pragma unroll
        for (int ks = 0; ks < 4; ++ks) qa[ks] = __builtin_amdgcn_raw_buffer_load_b128(rs, qpoff + ks * 64, 0, 16);
        if (wnone(bad16(qa[0]) | bad16(qa[1]) | bad16(qa[2]) | bad16(qa[3])) || ++spins > SPIN_CAP) break;
        __builtin_amdgcn_s_sleep(POLL_SLEEP);
      }
      STAMP(6);
      f32x4 d = zero4;
#pragma unroll
      for (int ks = 0; ks < 4; ++ks) d = mfma32(qa[ks], vk[ks], d);
      const float qv = ((d[0] + d[1]) + (d[2] + d[3])) * (0.08838834764831845f * 1.4426950408889634f);
      lds_st<u16>(L_QS + (g * 128 + 16 * w + l15) * 2, (u16)(pk2(qv, 0.f) & 0xFFFFu));
      BAR();
      if (w == 0) {
        v4u v = lds_ld<v4u>(L_QS + lane * 16);
        v.x = nosent(v.x); v.y = nosent(v.y); v.z = nosent(v.z); v.w = nosent(v.w);
        st16s(rs, WS_QB + (g * 32 + h) * 1024 + q4 * 256 + l15 * 16, v);
      }
    }
    STAMP(7);
    const int obase = WS_OB + (h * 4 + q4) * 32768;
#pragma unroll
    for (int i = 0; i < 4; ++i) st16s(rs, obase + (w * 4 + i) * 1024 + lane * 16, sent4);
    v4u fe[4][4];
    v4u ff[4][2];
    if (w < 2) {
      const rsrc_t rs_wuv = mkrs(p.wuv, DC * NH * HD * 2);
      v2u ve[4][8];
      const int off = (((128 * q4 + 4 * (g >> 1) + 16 * (g & 1)) * NH + h) * HD + 64 * w + 4 * l15) * 2;
#pragma unroll
      for (int ks = 0; ks < 4; ++ks)
#pragma unroll
        for (int i = 0; i < 8; ++i) ve[ks][i] = bl8(rs_wuv, off, (32 * ks + 8 * (i >> 2) + (i & 3)) * (NH * HD * 2));
      VMWAIT(32);
      BAR();
#pragma unroll
      for (int ks = 0; ks < 4; ++ks) tr8x4(ve[ks], fe[ks]);
    } else {
      VMWAIT(0);
      BAR();
      if (tid == 448) st4s(rs, WS_CF2 + (xg * 32 + slot) * 128, tag);
    }
    if (!isA) {
      unsigned vf[4][8];
      f_load(p, ft, w, l15, g, vf);
#pragma unroll
      for (int ks = 0; ks < 4; ++ks) tr8x2(vf[ks], ff[ks]);
    } else {
      const int mcol = (xg == 0 ? 2 : (xg == 1 ? 5 : (xg == 2 ? 3 : 4))) * DM + 32 * slot;
      v4u cf[3];
      unsigned va[3][8];
#pragma unroll
      for (int n = 0; n < 3; ++n) {
        const int kb = min(w + 8 * n, 19);
        cf[n] = bl16(rs_c, ((l15 >> 2) * DM + 32 * kb + 8 * g) * 2, 0);
        const int aoff = ((32 * kb + 8 * g) * (6 * DM) + mcol + 2 * l15) * 2;
#pragma unroll
        for (int i = 0; i < 8; ++i) va[n][i] = bl4(rs_wmod, aoff, i * (6 * DM * 2));
      }
      f32x4 ac[2] = {zero4, zero4};
#pragma unroll
      for (int n = 0; n < 3; ++n) {
        if (w + 8 * n < 20) {
          const v4u af = silu_frag(cf[n], lane & 1);
          v4u fa[2];
          tr8x2(va[n], fa);
          ac[0] = mfma32(af, fa[0], ac[0]);
          ac[1] = mfma32(af, fa[1], ac[1]);
        }
      }
      lds_st<v2u>(L_RA + ((w * 4 + g) * 32 + 2 * l15) * 4, v2u{__float_as_uint(ac[0][0] + ac[0][1]), __float_as_uint(ac[1][0] + ac[1][1])});
      BAR();
      if (w < 2) {
        const int b = tid >> 5, cl = tid & 31;
        float s = xg == 3 ? 1.f : 0.f;
#pragma unroll
        for (int ww = 0; ww < 8; ++ww) s += lds_ld<float>(L_RA + ((ww * 4 + b) * 32 + cl) * 4);
        st4s(rs, WS_MP + (xg * 4 + b) * 2560 + (32 * slot + cl) * 4, nosent(__float_as_uint(s)));
      }
    }
    if (w == 7) wait_flags<4>(rs, WS_CF1 + lane * 128, tag);
    BAR();
    STAMP(8);
    {
      const int b = w >> 1, sh = w & 1;
      unsigned spins = 0;
      v2u st;
      const int stoff = WS_ST + ((b * 32 + (lane & 31)) * 32 + h) * 8;
      const int ooff = obase + (b * 32 + 16 * sh + 4 * g) * 256 + l15 * 16;
      for (;;) {
        FENCE();
        st = ld8s(rs, stoff);
        if (wnone((unsigned)(max(st.x, st.y) == SENT)) || ++spins > SPIN_CAP) break;
        __builtin_amdgcn_s_sleep(4);
      }
      STAMP(9);
      const unsigned long long t_st = __builtin_amdgcn_s_memrealtime();
      v4u o[4];
      const float m = __uint_as_float(st.x), l = __uint_as_float(st.y);
      const float M = x16_max(max16(m));
      const float e = __builtin_amdgcn_exp2f(m - M);
      const float L = x16_sum(sum16(l * e));
      const float wt = e * __builtin_amdgcn_rcpf(L);
      lds_st<float>(L_W + w * 128 + (lane & 31) * 4, wt);
      const f32x4 w4 = lds_ld<f32x4>(L_W + w * 128 + (16 * sh + 4 * g) * 4);
      v2u aw;
      {
        const unsigned h01 = pk2(w4[0], w4[1]), h23 = pk2(w4[2], w4[3]);
        const unsigned l01 = pk2(w4[0] - bflo(h01), w4[1] - bfhi(h01)), l23 = pk2(w4[2] - bflo(h23), w4[3] - bfhi(h23));
        aw = sel2(l15 == 0, v2u{h01, h23}, sel2(l15 == 1, v2u{l01, l23}, v2u{0u, 0u}));
      }
      while (__builtin_amdgcn_s_memrealtime() - t_st < O_DELAY) __builtin_amdgcn_s_sleep(1);
      for (;;) {
        FENCE();
#pragma unroll
        for (int i = 0; i < 4; ++i) o[i] = __builtin_amdgcn_raw_buffer_load_b128(rs, ooff + i * 256, 0, 16);
        if (wnone(bad16(o[0]) | bad16(o[1]) | bad16(o[2]) | bad16(o[3])) || ++spins > SPIN_CAP) break;
        __builtin_amdgcn_s_sleep(POLL_SLEEP + 2);
      }
      STAMP(10);
      float cc[8];
#pragma unroll
      for (int q = 0; q < 4; ++q) {
        const v2u b0 = {plo(o[1][q], o[0][q]), plo(o[3][q], o[2][q])};
        const v2u b1 = {phi(o[1][q], o[0][q]), phi(o[3][q], o[2][q])};
        const f32x4 d0 = mfma16(aw, b0, zero4);
        const f32x4 d1 = mfma16(aw, b1, zero4);
        cc[2 * q] = d0[0] + d0[1];
        cc[2 * q + 1] = d1[0] + d1[1];
      }
      if (lane < 16) lds_st<v4u>(L_CC + (4 * b + sh) * 272 + lane * 16, v4u{pk2(cc[0], cc[1]), pk2(cc[2], cc[3]), pk2(cc[4], cc[5]), pk2(cc[6], cc[7])});
      BAR();
      if (w < 2) {
        f32x4 d[4] = {zero4, zero4, zero4, zero4};
        v4u a[4];
#pragma unroll
        for (int ks = 0; ks < 4; ++ks) a[ks] = lds_ld<v4u>(L_CC + l15 * 272 + (32 * ks + 8 * g) * 2);
        __builtin_amdgcn_sched_barrier(0);
#pragma unroll
        for (int ks = 0; ks < 4; ++ks)
#pragma unroll
          for (int j = 0; j < 4; ++j) d[j] = mfma32(a[ks], fe[ks][j], d[j]);
        st8s(rs, WS_PX + ((h * 4 + q4) * 4 + g) * 256 + (64 * w + 4 * l15) * 2,
             v2u{nosent(pk2(d[0][0] + d[0][1], d[1][0] + d[1][1])), nosent(pk2(d[2][0] + d[2][1], d[3][0] + d[3][1]))});
      }
    }
    STAMP(11);
    if (!isA) {
      f_run(rs, ft, tid, w, l15, g, ff, L_RF);
      STAMP(13);
      FLUSH_STAMPS();
      return;
    }
    float r2_x = 0.f, r2_gb = 0.f;
    if (w == 7) {
      const int b = (lane >> 3) & 3, j = lane & 7;
      unsigned spins = 0;
      unsigned a0, a1, a2, a3, ga, gb;
      const int o1 = (b * 640 + 8 * r + j) * 4;
      const unsigned xr = __builtin_amdgcn_raw_buffer_load_b16(rs_x, (b * DM + 8 * r + j) * 2, 0, 0);
      for (;;) {
        FENCE();
        a0 = ld4s(rs, WS_PO + 0 * 10240 + o1);
        a1 = ld4s(rs, WS_PO + 1 * 10240 + o1);
        a2 = ld4s(rs, WS_PO + 2 * 10240 + o1);
        a3 = ld4s(rs, WS_PO + 3 * 10240 + o1);
        ga = ld4s(rs, WS_MP + 0 * 10240 + o1);
        gb = ld4s(rs, WS_MP + 1 * 10240 + o1);
        if (wnone((unsigned)(max(max(max(a0, a1), max(a2, a3)), max(ga, gb)) == SENT)) || ++spins > SPIN_CAP) break;
        __builtin_amdgcn_s_sleep(16);
      }
      r2_gb = __uint_as_float(gb);
      r2_x = bf2f(xr) + __uint_as_float(ga) * ((__uint_as_float(a0) + __uint_as_float(a1)) + (__uint_as_float(a2) + __uint_as_float(a3)));
    } else {
      unsigned spins = 0;
      v4u t4;
      const int pdoff = WS_PD + r * 6912 + min(tid, 431) * 16;
      for (;;) {
        FENCE();
        t4 = ld16s(rs, pdoff);
        if (wnone(bad16(t4)) || ++spins > SPIN_CAP) break;
        __builtin_amdgcn_s_sleep(POLL_SLEEP + 2);
      }
      f32x4 v = __builtin_bit_cast(f32x4, t4);
      if (tid >= 432) v = zero4;
#pragma unroll
      for (int e = 0; e < 4; ++e) { float s = v[e]; s += dpp_ror8(s); s = x16_sum(s); s = x32_sum(s); v[e] = s; }
      if (lane < 8) lds_st<f32x4>(L_R2 + (w * 8 + lane) * 16, v);
    }
    STAMP(14);
    BAR();
    if (w == 7 && lane < 32) {
      const int b = lane >> 3, j = lane & 7;
      float s = 0.f;
#pragma unroll
      for (int ww = 0; ww < 7; ++ww) s += lds_ld<float>(L_R2 + ((ww * 8 + b * 2 + (j >> 2)) * 4 + (j & 3)) * 4);
      const float o = r2_x + r2_gb * s;
      *(GAS u16*)(p.out + b * DM + 8 * r + j) = (u16)(pk2(o, 0.f) & 0xFFFFu);
    }
    if (r == 0 && tid == 0) st4s(rs, WS_EPOCH, epoch + 1u);
    STAMP(15);
    FLUSH_STAMPS();
  } else {
    const int b = xg - 4, s = slot;
    const int gp = b * 14 + slot;
    const bool isG = slot < 14 && gp < 54;
    const bool isF = slot >= 14 && slot < 22;
    const int ft = 48 + b * 8 + (slot - 14);
    constexpr unsigned L_KV = 0, L_Q = 131072;
    constexpr unsigned L_EX = 0;
    constexpr unsigned L_MX = 16384;
    constexpr unsigned L_SM = 17408;
    constexpr unsigned L_P = 20480;
    if (w == 7) {
      if (lane < 16) st16s(rs, WS_ST + (b * 32 + s) * 256 + lane * 16, sent4);
      if (isF) f_clear(rs, ft, lane);
      if (isG) {
        const int o0 = WS_PD + (lane >> 3) * 6912 + gp * 128 + (lane & 7) * 16;
#pragma unroll
        for (int i = 0; i < 10; ++i) __builtin_amdgcn_raw_buffer_store_b128(sent4, rs, o0, i * 8 * 6912, 16);
      }
      VMWAIT(0);
      if (lane == 0) st4s(rs, WS_CF1 + blockIdx.x * 128, tag);
    }
    if (KV_DELAY) { while (__builtin_amdgcn_s_memrealtime() - t_start < KV_DELAY) __builtin_amdgcn_s_sleep(2); }
    {
      const u16* kvb = p.ckv + ((size_t)b * SK + s * 128 + 16 * w) * DC;
#pragma unroll
      for (int i = 0; i < 16; ++i) {
        const u16* src = kvb + (size_t)i * DC + ((lane ^ i) * 8);
        unsigned keep;
        asm volatile("s_mov_b32 %0, m0\n\ts_mov_b32 m0, %2\n\ts_nop 0\n\tglobal_load_lds_dwordx4 %1, off nt\n\ts_mov_b32 m0, %0"
                     : "=&s"(keep) : "v"(src), "s"(L_KV + (16 * w + i) * 1024) : "memory");
      }
    }
    VMWAIT(0);
    STAMP(1);
    BAR();
    const int t = w >> 1, ch = w & 1;
    const int goff = (g == 0) ? 0 : (g == 1 ? 16 : (g == 2 ? 8 : 24));
    v4u kf[8][2];
#pragma unroll
    for (int ss = 0; ss < 8; ++ss)
#pragma unroll
      for (int kt = 0; kt < 2; ++kt)
        kf[ss][kt] = lds_ld<v4u>(L_KV + (32 * t + 16 * kt + l15) * 1024 + (((32 * ch + ss + goff) ^ l15) * 16));
#pragma unroll
    for (int ss = 0; ss < 8; ++ss) asm volatile("" : "+v"(kf[ss][0]), "+v"(kf[ss][1]));
    v2u vt[4][4][2];
    {
      const int q = l15 >> 2, pp = lane & 3;
#pragma unroll
      for (int t2 = 0; t2 < 2; ++t2) {
        const int row = 8 * g + 4 * t2 + q;
        const unsigned base = L_KV + row * 1024 + 8 * (pp & 1);
#pragma unroll
        for (int j = 0; j < 4; ++j) {
          const unsigned addr = base + (((8 * w + j + 4 * (pp >> 1)) ^ (row & 15)) * 16);
          const unsigned addr2 = addr + 65536;
          asm volatile("ds_read_b64_tr_b16 %0, %1" : "=v"(vt[0][j][t2]) : "v"(addr) : "memory");
          asm volatile("ds_read_b64_tr_b16 %0, %1 offset:32768" : "=v"(vt[1][j][t2]) : "v"(addr) : "memory");
          asm volatile("ds_read_b64_tr_b16 %0, %1" : "=v"(vt[2][j][t2]) : "v"(addr2) : "memory");
          asm volatile("ds_read_b64_tr_b16 %0, %1 offset:32768" : "=v"(vt[3][j][t2]) : "v"(addr2) : "memory");
        }
      }
    }
    if (w == 7) wait_flags<4>(rs, WS_CF1 + lane * 128, tag);
    BAR();
#pragma unroll
    for (int ks = 0; ks < 4; ++ks)
      asm volatile("" : "+v"(vt[ks][0][0]), "+v"(vt[ks][0][1]), "+v"(vt[ks][1][0]), "+v"(vt[ks][1][1]), "+v"(vt[ks][2][0]), "+v"(vt[ks][2][1]), "+v"(vt[ks][3][0]), "+v"(vt[ks][3][1]));
    STAMP(8);
    {
      unsigned spins = 0;
      v4u q[4];
      const int qoff = WS_QB + b * 32768 + w * 4096 + lane * 16;
      for (;;) {
        FENCE();
#pragma unroll
        for (int i = 0; i < 4; ++i) q[i] = __builtin_amdgcn_raw_buffer_load_b128(rs, qoff + i * 1024, 0, 16);
        if (wnone(bad16(q[0]) | bad16(q[1]) | bad16(q[2]) | bad16(q[3])) || ++spins > SPIN_CAP) break;
        __builtin_amdgcn_s_sleep(POLL_SLEEP + 2);
      }
      const int c5 = lane & 31, ss = c5 & 7, tt = c5 >> 3, gg = ((tt & 1) << 1) | (tt >> 1);
      const unsigned qb = L_Q + (lane >> 5) * 16384 + ss * 2048 + (w >> 2) * 1024 + gg * 256;
#pragma unroll
      for (int i = 0; i < 4; ++i) lds_st<v4u>(qb + ((((4 * w + i) & 15) ^ ss) * 16), q[i]);
    }
    BAR();
    STAMP(2);
    {
      f32x4 acc[2][2] = {{zero4, zero4}, {zero4, zero4}};
      {
        v4u qf[8][2];
#pragma unroll
        for (int ss = 0; ss < 8; ++ss)
#pragma unroll
          for (int ht = 0; ht < 2; ++ht) qf[ss][ht] = lds_ld<v4u>(L_Q + ch * 16384 + ss * 2048 + ht * 1024 + g * 256 + ((l15 ^ ss) * 16));
        __builtin_amdgcn_sched_barrier(0);
#pragma unroll
        for (int ss = 0; ss < 8; ++ss)
#pragma unroll
          for (int kt = 0; kt < 2; ++kt) {
            acc[kt][0] = mfma32(kf[ss][kt], qf[ss][0], acc[kt][0]);
            acc[kt][1] = mfma32(kf[ss][kt], qf[ss][1], acc[kt][1]);
          }
      }
#pragma unroll
      for (int ht = 0; ht < 2; ++ht) lds_st<f32x4>(L_EX + (w * 2 + ht) * 1024 + lane * 16, sel4(ch != 0, acc[0][ht], acc[1][ht]));
      BAR();
      STAMP(11);
      f32x4 sc[2];
#pragma unroll
      for (int ht = 0; ht < 2; ++ht) sc[ht] = sel4(ch != 0, acc[1][ht], acc[0][ht]) + lds_ld<f32x4>(L_EX + ((w ^ 1) * 2 + ht) * 1024 + lane * 16);
#pragma unroll
      for (int ht = 0; ht < 2; ++ht) {
        float m = fmaxf(fmaxf(sc[ht][0], sc[ht][1]), fmaxf(sc[ht][2], sc[ht][3]));
        m = x32_max(x16_max(m));
        if (lane < 16) lds_st<float>(L_MX + ((ht * 16 + lane) * 8 + w) * 4, m);
      }
      BAR();
      STAMP(12);
#pragma unroll
      for (int ht = 0; ht < 2; ++ht) {
        const f32x4 a = lds_ld<f32x4>(L_MX + (ht * 16 + l15) * 32), c = lds_ld<f32x4>(L_MX + (ht * 16 + l15) * 32 + 16);
        const float mx = fmaxf(fmaxf(fmaxf(a[0], a[1]), fmaxf(a[2], a[3])), fmaxf(fmaxf(c[0], c[1]), fmaxf(c[2], c[3])));
        float pr[4];
#pragma unroll
        for (int e = 0; e < 4; ++e) pr[e] = __builtin_amdgcn_exp2f(sc[ht][e] - mx);
        const v2u pk = {pk2(pr[0], pr[1]), pk2(pr[2], pr[3])};
        float sm = (bflo(pk.x) + bfhi(pk.x)) + (bflo(pk.y) + bfhi(pk.y));
        sm = x32_sum(x16_sum(sm));
        if (lane < 16) lds_st<float>(L_SM + ((ht * 16 + lane) * 8 + w) * 4, sm);
        lds_st<v2u>(L_P + ((ht * 4 + (w >> 1)) * 64 + (2 * (w & 1) + (g >> 1)) * 16 + l15) * 16 + 8 * (g & 1), pk);
      }
      if (w == 7) wait_flags<2>(rs, WS_CF2 + lane * 128, tag);
      BAR();
      STAMP(3);
      v4u pf[4][2];
#pragma unroll
      for (int ks = 0; ks < 4; ++ks)
#pragma unroll
        for (int ht = 0; ht < 2; ++ht) pf[ks][ht] = lds_ld<v4u>(L_P + ((ht * 4 + ks) * 64 + lane) * 16);
      __builtin_amdgcn_sched_barrier(0);
      if (w == 0 && lane < 32) {
        const f32x4 a = lds_ld<f32x4>(L_SM + lane * 32), c = lds_ld<f32x4>(L_SM + lane * 32 + 16);
        const float l = ((a[0] + a[1]) + (a[2] + a[3])) + ((c[0] + c[1]) + (c[2] + c[3]));
        const f32x4 a2 = lds_ld<f32x4>(L_MX + lane * 32), c2 = lds_ld<f32x4>(L_MX + lane * 32 + 16);
        const float m = fmaxf(fmaxf(fmaxf(a2[0], a2[1]), fmaxf(a2[2], a2[3])), fmaxf(fmaxf(c2[0], c2[1]), fmaxf(c2[2], c2[3])));
        st8s(rs, WS_ST + ((b * 32 + s) * 32 + lane) * 8, v2u{nosent(__float_as_uint(m)), nosent(__float_as_uint(l))});
      }
      f32x4 oc[4][2];
#pragma unroll
      for (int j = 0; j < 4; ++j) { oc[j][0] = zero4; oc[j][1] = zero4; }
#pragma unroll
      for (int ks = 0; ks < 4; ++ks)
#pragma unroll
        for (int j = 0; j < 4; ++j) {
          const v4u a = {vt[ks][j][0].x, vt[ks][j][0].y, vt[ks][j][1].x, vt[ks][j][1].y};
          oc[j][0] = mfma32(a, pf[ks][0], oc[j][0]);
          oc[j][1] = mfma32(a, pf[ks][1], oc[j][1]);
        }
      STAMP(9);
      const bool odd = lane & 1;
      const int oeven = WS_OB + (((l15 & ~1) * 4 + (w >> 1)) * 128 + b * 32 + s) * 256 + ((w & 1) * 8 + g * 2 + (lane & 1)) * 16;
#pragma unroll
      for (int ht = 0; ht < 2; ++ht) {
        v4u X, Y, S1, S2;
        X = v4u{pk2(oc[0][ht][0], oc[0][ht][1]), pk2(oc[0][ht][2], oc[0][ht][3]), pk2(oc[1][ht][0], oc[1][ht][1]), pk2(oc[1][ht][2], oc[1][ht][3])};
        Y = v4u{pk2(oc[2][ht][0], oc[2][ht][1]), pk2(oc[2][ht][2], oc[2][ht][3]), pk2(oc[3][ht][0], oc[3][ht][1]), pk2(oc[3][ht][2], oc[3][ht][3])};
#pragma unroll
        for (int e = 0; e < 4; ++e) {
          const unsigned T = odd ? X[e] : Y[e];
          const unsigned R = (unsigned)__builtin_amdgcn_mov_dpp((int)T, 0xB1, 0xf, 0xf, true);
          S1[e] = nosent(odd ? R : X[e]);
          S2[e] = nosent(odd ? Y[e] : R);
        }
        __builtin_amdgcn_raw_buffer_store_b128(S1, rs, oeven, ht * (16 * 4 * 128 * 256), 16);
        __builtin_amdgcn_raw_buffer_store_b128(S2, rs, oeven + 4 * 128 * 256, ht * (16 * 4 * 128 * 256), 16);
      }
    }
    STAMP(4);
    if (isF) {
      unsigned vf[4][8];
      f_load(p, ft, w, l15, g, vf);
      v4u ff[4][2];
#pragma unroll
      for (int ks = 0; ks < 4; ++ks) tr8x2(vf[ks], ff[ks]);
      BAR();
      STAMP(5);
      f_run(rs, ft, tid, w, l15, g, ff, 0);
      STAMP(7);
      FLUSH_STAMPS();
      return;
    }
    if (!isG) { FLUSH_STAMPS(); return; }
    constexpr unsigned L_GA = 0;
    constexpr unsigned L_RG = 21504;
    constexpr unsigned L_LN = 38912;
    constexpr unsigned L_HH = 39168;
    v4u fg[3][4];
    {
      v2u vg[3][8];
#pragma unroll
      for (int n = 0; n < 3; ++n) {
        const int kb = min(w + 8 * n, 19);
        const u16* src = ((l15 >> 3) ? p.wu : p.wg) + (size_t)(32 * kb + 8 * g) * DF + 32 * gp + 4 * (l15 & 7);
#pragma unroll
        for (int i = 0; i < 8; ++i) vg[n][i] = ldg<v2u>(src + (size_t)i * DF);
      }
#pragma unroll
      for (int n = 0; n < 3; ++n) tr8x4(vg[n], fg[n]);
    }
    v4u fd[2][4];
    {
      const rsrc_t rs_wd = mkrs(p.wd, DF * DM * 2);
      v2u vd[2][8];
#pragma unroll
      for (int n = 0; n < 2; ++n) {
        const int u = min(w + 8 * n, 9);
        const int off = ((32 * gp + 8 * g) * DM + 64 * u + 4 * l15) * 2;
#pragma unroll
        for (int i = 0; i < 8; ++i) vd[n][i] = bl8(rs_wd, off, i * (DM * 2));
      }
#pragma unroll
      for (int n = 0; n < 2; ++n) tr8x4(vd[n], fd[n]);
    }
    BAR();
    const int gb_ = w >> 1;
    const rsrc_t rs_x = mkrs(p.x, NB * DM * 2);
    f32x4 ga4[2], cb4[2];
    v2u x4[2];
    int m0[2];
    m0[0] = 4 * (80 * (w & 1) + lane);
    m0[1] = 4 * (80 * (w & 1) + 64 + (lane & 15));
    {
      v4u a[2], c2[2], s2[2];
      unsigned spins = 0;
#pragma unroll
      for (int n = 0; n < 2; ++n) x4[n] = bl8(rs_x, (gb_ * DM + m0[n]) * 2, 0);
      for (;;) {
        FENCE();
        unsigned bad = 0;
#pragma unroll
        for (int n = 0; n < 2; ++n) {
          a[n] = ld16s(rs, WS_MP + (0 * 4 + gb_) * 2560 + m0[n] * 4);
          s2[n] = ld16s(rs, WS_MP + (2 * 4 + gb_) * 2560 + m0[n] * 4);
          c2[n] = ld16s(rs, WS_MP + (3 * 4 + gb_) * 2560 + m0[n] * 4);
        }
#pragma unroll
        for (int n = 0; n < 2; ++n) bad |= bad16(a[n]) | bad16(s2[n]) | bad16(c2[n]);
        if (wnone(bad) || ++spins > SPIN_CAP) break;
        __builtin_amdgcn_s_sleep(16);
      }
#pragma unroll
      for (int n = 0; n < 2; ++n) {
        ga4[n] = __builtin_bit_cast(f32x4, a[n]);
        cb4[n] = __builtin_bit_cast(f32x4, c2[n]);
        const f32x4 sb = __builtin_bit_cast(f32x4, s2[n]);
        lds_st<v2u>(L_GA + (4 * gb_ + 2) * 1296 + m0[n] * 2, v2u{pk2(cb4[n][0], cb4[n][1]), pk2(cb4[n][2], cb4[n][3])});
        lds_st<v2u>(L_GA + (4 * gb_ + 3) * 1296 + m0[n] * 2, v2u{pk2(sb[0], sb[1]), pk2(sb[2], sb[3])});
      }
    }
    BAR();
    {
      f32x4 ac[4] = {zero4, zero4, zero4, zero4};
      v4u aa[3];
#pragma unroll
      for (int n = 0; n < 3; ++n) aa[n] = lds_ld<v4u>(L_GA + l15 * 1296 + (32 * min(w + 8 * n, 19) + 8 * g) * 2);
      __builtin_amdgcn_sched_barrier(0);
#pragma unroll
      for (int n = 0; n < 3; ++n) {
        if (w + 8 * n < 20) {
#pragma unroll
          for (int j = 0; j < 4; ++j) ac[j] = mfma32(aa[n], fg[n][j], ac[j]);
        }
      }
      lds_st<f32x4>(L_RG + (((w * 2 + 0) * 4 + g) * 64 + 4 * l15) * 4, f32x4{ac[0][2], ac[1][2], ac[2][2], ac[3][2]});
      lds_st<f32x4>(L_RG + (((w * 2 + 1) * 4 + g) * 64 + 4 * l15) * 4, f32x4{ac[0][3], ac[1][3], ac[2][3], ac[3][3]});
    }
    BAR();
    float U1 = 0.f, U2 = 0.f;
    if (w < 4) {
#pragma unroll
      for (int ww = 0; ww < 8; ++ww) {
        U1 += lds_ld<float>(L_RG + (((ww * 2 + 0) * 4 + w) * 64 + lane) * 4);
        U2 += lds_ld<float>(L_RG + (((ww * 2 + 1) * 4 + w) * 64 + lane) * 4);
      }
    }
    BAR();
    STAMP(5);
    {
      float s1 = 0.f, s2 = 0.f;
      v4u a[2][4];
      unsigned spins = 0;
      for (;;) {
        FENCE();
        unsigned bad = 0;
#pragma unroll
        for (int n = 0; n < 2; ++n)
#pragma unroll
          for (int kr = 0; kr < 4; ++kr) a[n][kr] = __builtin_amdgcn_raw_buffer_load_b128(rs, WS_PO + gb_ * 2560 + m0[n] * 4, kr * 10240, 16);
#pragma unroll
        for (int n = 0; n < 2; ++n)
#pragma unroll
          for (int kr = 0; kr < 4; ++kr) bad |= bad16(a[n][kr]);
        if (wnone(bad) || ++spins > SPIN_CAP) break;
        __builtin_amdgcn_s_sleep(POLL_SLEEP + 2);
      }
      STAMP(6);
#pragma unroll
      for (int n = 0; n < 2; ++n) {
        const f32x4 ao = (__builtin_bit_cast(f32x4, a[n][0]) + __builtin_bit_cast(f32x4, a[n][1])) + (__builtin_bit_cast(f32x4, a[n][2]) + __builtin_bit_cast(f32x4, a[n][3]));
        const float xv[4] = {bflo(x4[n].x), bfhi(x4[n].x), bflo(x4[n].y), bfhi(x4[n].y)};
        float y[4];
        const float live = (n == 0 || lane < 16) ? 1.f : 0.f;
#pragma unroll
        for (int e = 0; e < 4; ++e) {
          const float x2 = xv[e] + ga4[n][e] * ao[e];
          s1 += live * x2; s2 += live * x2 * x2;
          y[e] = x2 * cb4[n][e];
        }
        const unsigned h01 = pk2(y[0], y[1]), h23 = pk2(y[2], y[3]);
        const unsigned l01 = pk2(y[0] - bflo(h01), y[1] - bfhi(h01)), l23 = pk2(y[2] - bflo(h23), y[3] - bfhi(h23));
        lds_st<v2u>(L_GA + (4 * gb_ + 0) * 1296 + m0[n] * 2, v2u{h01, h23});
        lds_st<v2u>(L_GA + (4 * gb_ + 1) * 1296 + m0[n] * 2, v2u{l01, l23});
      }
      s1 = x32_sum(x16_sum(sum16(s1)));
      s2 = x32_sum(x16_sum(sum16(s2)));
      if (lane == 0) lds_st<v2u>(L_LN + w * 8, v2u{__float_as_uint(s1), __float_as_uint(s2)});
    }
    BAR();
    {
      f32x4 ac[4] = {zero4, zero4, zero4, zero4};
      v4u aa[3];
#pragma unroll
      for (int n = 0; n < 3; ++n) aa[n] = lds_ld<v4u>(L_GA + l15 * 1296 + (32 * min(w + 8 * n, 19) + 8 * g) * 2);
      __builtin_amdgcn_sched_barrier(0);
#pragma unroll
      for (int n = 0; n < 3; ++n) {
        if (w + 8 * n < 20) {
#pragma unroll
          for (int j = 0; j < 4; ++j) ac[j] = mfma32(aa[n], fg[n][j], ac[j]);
        }
      }
      lds_st<f32x4>(L_RG + (((w * 2 + 0) * 4 + g) * 64 + 4 * l15) * 4, f32x4{ac[0][0] + ac[0][1], ac[1][0] + ac[1][1], ac[2][0] + ac[2][1], ac[3][0] + ac[3][1]});
    }
    BAR();
    if (w < 4) {
      float T = 0.f;
#pragma unroll
      for (int ww = 0; ww < 8; ++ww) T += lds_ld<float>(L_RG + (((ww * 2 + 0) * 4 + w) * 64 + lane) * 4);
      const v4u ln = lds_ld<v4u>(L_LN + w * 16);
      const float s1 = __uint_as_float(ln.x) + __uint_as_float(ln.z), s2 = __uint_as_float(ln.y) + __uint_as_float(ln.w);
      const float mean = s1 * (1.f / DM);
      const float rstd = rsqrtf(fmaxf(s2 * (1.f / DM) - mean * mean, 0.f) + 1e-5f);
      const float val = rstd * (T - mean * U1) + U2;
      auto sw = __builtin_amdgcn_permlane32_swap(__float_as_uint(val), __float_as_uint(val), false, false);
      const float gv = __uint_as_float(sw[0]), uv = __uint_as_float(sw[1]);
      const float z = 0.7978845608028654f * (gv + 0.044715f * gv * gv * gv);
      const float th = 1.f - 2.f * __builtin_amdgcn_rcpf(1.f + __expf(2.f * z));
      const float hh = 0.5f * gv * (1.f + th) * uv;
      const unsigned hi = pk2(hh, 0.f) & 0xFFFFu;
      const unsigned lo = pk2(hh - bf2f(hi), 0.f) & 0xFFFFu;
      lds_st<u16>(L_HH + (4 * w + (lane >> 5)) * 80 + (lane & 31) * 2, (u16)(lane < 32 ? hi : lo));
    }
    BAR();
    {
      const v4u a = lds_ld<v4u>(L_HH + l15 * 80 + g * 16);
#pragma unroll
      for (int n = 0; n < 2; ++n) {
        const int u = w + 8 * n;
        if (u < 10) {
          f32x4 o;
#pragma unroll
          for (int j = 0; j < 4; ++j) { const f32x4 d = mfma32(a, fd[n][j], zero4); o[j] = d[0] + d[1]; }
          const v4u ov = {nosent(__float_as_uint(o[0])), nosent(__float_as_uint(o[1])), nosent(__float_as_uint(o[2])), nosent(__float_as_uint(o[3]))};
          st16s(rs, WS_PD + (8 * u + (l15 >> 1)) * 6912 + gp * 128 + g * 32 + (l15 & 1) * 16, ov);
        }
      }
    }
    STAMP(7);
    FLUSH_STAMPS();
  }
}

extern "C" void kernel_launch(void* const* d_in, const int* in_sizes, int n_in, void* d_out, int out_size, void* d_ws, size_t ws_size, hipStream_t stream) {
  Params p;
  p.x = (const u16*)d_in[0]; p.ckv = (const u16*)d_in[1]; p.wq = (const u16*)d_in[2]; p.wuk = (const u16*)d_in[3];
  p.wuv = (const u16*)d_in[4]; p.wo = (const u16*)d_in[5]; p.wg = (const u16*)d_in[7]; p.wu = (const u16*)d_in[8];
  p.wd = (const u16*)d_in[9]; p.c = (const u16*)d_in[10]; p.wmod = (const u16*)d_in[11];
  p.out = (u16*)d_out; p.ws = (unsigned char*)d_ws;
  (void)hipFuncSetAttribute((const void*)mega, hipFuncAttributeMaxDynamicSharedMemorySize, LDS_BYTES);
  hipLaunchKernelGGL(mega, dim3(256), dim3(512), LDS_BYTES, stream, p);
}
```

```cpp
#include <hip/hip_runtime.h>
#include <stdint.h>

typedef unsigned short u16;
typedef unsigned v4u __attribute__((ext_vector_type(4)));
typedef unsigned v2u __attribute__((ext_vector_type(2)));
typedef float f32x4 __attribute__((ext_vector_type(4)));
typedef __bf16 bf16x8 __attribute__((ext_vector_type(8)));
typedef short s16x4 __attribute__((ext_vector_type(4)));
#define GAS __attribute__((address_space(1)))
#define LAS __attribute__((address_space(3)))
#define DEV __device__ __forceinline__

#define ROT 7
#define H1_T 340
#define O_DELAY 30
#define KV_DELAY 150
#define POLL_SLEEP 1
#define STAMPS 0

constexpr int NB = 4, NH = 32, SK = 4096, HD = 128, DM = 640, DC = 512, DF = 1728;
constexpr unsigned SENT = 0xFFFFFFFFu;
constexpr unsigned SPIN_CAP = 1u << 16;

constexpr int WS_EPOCH = 0;
constexpr int WS_CF1 = 0x180000 + 0x40000;
constexpr int WS_CF2 = 0x180000 + 0x50000;
constexpr int WS_HB = 0x100000;
constexpr int WS_QP = 0x10000;
constexpr int WS_QB = 0x30000;
constexpr int WS_ST = 0x50000;
constexpr int WS_PX = 0x60000;
constexpr int WS_PO = 0x80000;
constexpr int WS_MP = 0x90000;
constexpr int WS_PD = 0x120000;
constexpr int WS_OB = 0x200000;
constexpr int WS_STAMP = 0x800000;
constexpr int WS_BYTES = 0x1000000;
constexpr int LDS_BYTES = 163840;

struct Params {
  const u16 *x, *ckv, *wq, *wuk, *wuv, *wo, *wg, *wu, *wd, *c, *wmod;
  u16* out;
  unsigned char* ws;
};

DEV float bf2f(unsigned b) { return __uint_as_float(b << 16); }
DEV float bflo(unsigned w) { return __uint_as_float(w << 16); }
DEV float bfhi(unsigned w) { return __uint_as_float(w & 0xFFFF0000u); }
DEV unsigned pk2(float lo, float hi) {
  typedef __bf16 bf2 __attribute__((ext_vector_type(2)));
  typedef float f2 __attribute__((ext_vector_type(2)));
  f2 v = {lo, hi};
  bf2 r = __builtin_convertvector(v, bf2);
  return __builtin_bit_cast(unsigned, r);
}
DEV unsigned nosent(unsigned v) { return v < 0xFFFFFFFEu ? v : 0xFFFFFFFEu; }
DEV f32x4 mfma32(v4u a, v4u b, f32x4 c) {
  return __builtin_amdgcn_mfma_f32_16x16x32_bf16(__builtin_bit_cast(bf16x8, a), __builtin_bit_cast(bf16x8, b), c, 0, 0, 0);
}
DEV f32x4 mfma16(v2u a, v2u b, f32x4 c) {
  return __builtin_amdgcn_mfma_f32_16x16x16bf16_1k(__builtin_bit_cast(s16x4, a), __builtin_bit_cast(s16x4, b), c, 0, 0, 0);
}
constexpr unsigned PLO = 0x05040100u, PHI = 0x07060302u;
DEV unsigned plo(unsigned hi_row, unsigned lo_row) { return __builtin_amdgcn_perm(hi_row, lo_row, PLO); }
DEV unsigned phi(unsigned hi_row, unsigned lo_row) { return __builtin_amdgcn_perm(hi_row, lo_row, PHI); }
DEV void tr8x8(const v4u (&v)[8], v4u (&f)[8]) {
#pragma unroll
  for (int q = 0; q < 4; ++q) {
#pragma unroll
    for (int t = 0; t < 4; ++t) {
      f[2 * q][t] = plo(v[2 * t + 1][q], v[2 * t][q]);
      f[2 * q + 1][t] = phi(v[2 * t + 1][q], v[2 * t][q]);
    }
  }
}
DEV void tr8x4(const v2u (&v)[8], v4u (&f)[4]) {
#pragma unroll
  for (int q = 0; q < 2; ++q) {
#pragma unroll
    for (int t = 0; t < 4; ++t) {
      f[2 * q][t] = plo(v[2 * t + 1][q], v[2 * t][q]);
      f[2 * q + 1][t] = phi(v[2 * t + 1][q], v[2 * t][q]);
    }
  }
}
DEV void tr8x2(const unsigned (&v)[8], v4u (&f)[2]) {
#pragma unroll
  for (int t = 0; t < 4; ++t) {
    f[0][t] = plo(v[2 * t + 1], v[2 * t]);
    f[1][t] = phi(v[2 * t + 1], v[2 * t]);
  }
}
DEV unsigned tagf(unsigned e) {
  e += 0x9E3779B9u; e ^= e >> 16; e *= 0x85EBCA6Bu; e ^= e >> 13; e *= 0xC2B2AE35u; e ^= e >> 16; return e;
}
#define BAR() asm volatile("s_waitcnt lgkmcnt(0)\n\ts_barrier" ::: "memory")
#define VMWAIT(n) asm volatile("s_waitcnt vmcnt(" #n ")" ::: "memory")
#define FENCE() asm volatile("" ::: "memory")
typedef __amdgpu_buffer_rsrc_t rsrc_t;
DEV v4u ld16s(rsrc_t rs, int off) { return __builtin_amdgcn_raw_buffer_load_b128(rs, off, 0, 16); }
DEV v2u ld8s(rsrc_t rs, int off) { return __builtin_amdgcn_raw_buffer_load_b64(rs, off, 0, 16); }
DEV unsigned ld4s(rsrc_t rs, int off) { return __builtin_amdgcn_raw_buffer_load_b32(rs, off, 0, 16); }
DEV void st16s(rsrc_t rs, int off, v4u v) { __builtin_amdgcn_raw_buffer_store_b128(v, rs, off, 0, 16); }
DEV void st8s(rsrc_t rs, int off, v2u v) { __builtin_amdgcn_raw_buffer_store_b64(v, rs, off, 0, 16); }
DEV void st4s(rsrc_t rs, int off, unsigned v) { __builtin_amdgcn_raw_buffer_store_b32(v, rs, off, 0, 16); }
DEV bool ok16(v4u v) {
  unsigned m = max(max(v.x, v.y), max(v.z, v.w));
  return m != SENT;
}
DEV bool ok8(v2u v) { return max(v.x, v.y) != SENT; }
template <typename T> DEV T ldg(const void* p) { return *(const GAS T*)p; }
template <typename T> DEV T ldnt(const void* p) { return __builtin_nontemporal_load((const GAS T*)p); }
template <typename T> DEV T lds_ld(unsigned off) { return *(const LAS T*)(uintptr_t)off; }
template <typename T> DEV void lds_st(unsigned off, T v) { *(LAS T*)(uintptr_t)off = v; }

DEV float dpp_xor1(float v) { return __builtin_bit_cast(float, __builtin_amdgcn_mov_dpp(__builtin_bit_cast(int, v), 0xB1, 0xf, 0xf, true)); }
DEV float dpp_xor2(float v) { return __builtin_bit_cast(float, __builtin_amdgcn_mov_dpp(__builtin_bit_cast(int, v), 0x4E, 0xf, 0xf, true)); }
DEV float dpp_ror4(float v) { return __builtin_bit_cast(float, __builtin_amdgcn_mov_dpp(__builtin_bit_cast(int, v), 0x124, 0xf, 0xf, true)); }
DEV float dpp_ror8(float v) { return __builtin_bit_cast(float, __builtin_amdgcn_mov_dpp(__builtin_bit_cast(int, v), 0x128, 0xf, 0xf, true)); }
DEV float sum16(float v) { v += dpp_xor1(v); v += dpp_xor2(v); v += dpp_ror4(v); v += dpp_ror8(v); return v; }
DEV float x16_sum(float x) {
  auto s = __builtin_amdgcn_permlane16_swap(__float_as_uint(x), __float_as_uint(x), false, false);
  return __uint_as_float(s[0]) + __uint_as_float(s[1]);
}
DEV float x32_sum(float x) {
  auto t = __builtin_amdgcn_permlane32_swap(__float_as_uint(x), __float_as_uint(x), false, false);
  return __uint_as_float(t[0]) + __uint_as_float(t[1]);
}
DEV float x16_max(float x) {
  auto s = __builtin_amdgcn_permlane16_swap(__float_as_uint(x), __float_as_uint(x), false, false);
  return fmaxf(__uint_as_float(s[0]), __uint_as_float(s[1]));
}
DEV float x32_max(float x) {
  auto t = __builtin_amdgcn_permlane32_swap(__float_as_uint(x), __float_as_uint(x), false, false);
  return fmaxf(__uint_as_float(t[0]), __uint_as_float(t[1]));
}
DEV float max16(float v) { v = fmaxf(v, dpp_xor1(v)); v = fmaxf(v, dpp_xor2(v)); v = fmaxf(v, dpp_ror4(v)); v = fmaxf(v, dpp_ror8(v)); return v; }
DEV f32x4 sel4(bool c, f32x4 a, f32x4 b) { f32x4 r; r[0] = c ? a[0] : b[0]; r[1] = c ? a[1] : b[1]; r[2] = c ? a[2] : b[2]; r[3] = c ? a[3] : b[3]; return r; }
DEV v2u sel2(bool c, v2u a, v2u b) { v2u r; r[0] = c ? a[0] : b[0]; r[1] = c ? a[1] : b[1]; return r; }
DEV bool wall(bool p) { return __builtin_amdgcn_ballot_w64(!p) == 0ull; }

#define STAMP(i) do { } while (0)
#define FLUSH_STAMPS() do { } while (0)
DEV rsrc_t mkrs(const void* ptr, int bytes) { return __builtin_amdgcn_make_buffer_rsrc((void*)ptr, 0, bytes, 0x00020000); }
DEV v4u bl16(rsrc_t r, int voff, int soff) { return __builtin_amdgcn_raw_buffer_load_b128(r, voff, soff, 0); }
DEV v4u bl16nt(rsrc_t r, int voff, int soff) { return __builtin_amdgcn_raw_buffer_load_b128(r, voff, soff, 2); }
DEV v2u bl8(rsrc_t r, int voff, int soff) { return __builtin_amdgcn_raw_buffer_load_b64(r, voff, soff, 0); }
DEV unsigned bl4(rsrc_t r, int voff, int soff) { return __builtin_amdgcn_raw_buffer_load_b32(r, voff, soff, 0); }
DEV unsigned bad16(v4u v) { return (unsigned)(max(max(v.x, v.y), max(v.z, v.w)) == SENT); }
DEV bool wnone(unsigned bad) { return __builtin_amdgcn_ballot_w64(bad != 0u) == 0ull; }
DEV float silu(float v) { return v * __builtin_amdgcn_rcpf(1.f + __expf(-v)); }
DEV v4u silu_frag(v4u cv, bool lo_lane) {
  v4u o;
#pragma unroll
  for (int q = 0; q < 4; ++q) {
    const float a = silu(bflo(cv[q])), b = silu(bfhi(cv[q]));
    const unsigned hi = pk2(a, b);
    const unsigned lo = pk2(a - bflo(hi), b - bfhi(hi));
    o[q] = lo_lane ? lo : hi;
  }
  return o;
}

template <int NL> DEV void wait_flags(rsrc_t rs, int off, unsigned tag) {
  unsigned spins = 0;
  for (;;) {
    FENCE();
    unsigned bad = 0;
    unsigned v[NL];
#pragma unroll
    for (int i = 0; i < NL; ++i) v[i] = __builtin_amdgcn_raw_buffer_load_b32(rs, off, i * 8192, 16);
#pragma unroll
    for (int i = 0; i < NL; ++i) bad |= v[i] ^ tag;
    if (wnone(bad) || ++spins > SPIN_CAP) break;
    __builtin_amdgcn_s_sleep(6);
  }
}

template <bool NT> DEV void dma16(const void* gsrc, unsigned lds_base) {
  unsigned keep;
  if (NT) asm volatile("s_mov_b32 %0, m0\n\ts_mov_b32 m0, %2\n\ts_nop 0\n\tglobal_load_lds_dwordx4 %1, off nt\n\ts_mov_b32 m0, %0" : "=&s"(keep) : "v"(gsrc), "s"(lds_base) : "memory");
  else asm volatile("s_mov_b32 %0, m0\n\ts_mov_b32 m0, %2\n\ts_nop 0\n\tglobal_load_lds_dwordx4 %1, off\n\ts_mov_b32 m0, %0" : "=&s"(keep) : "v"(gsrc), "s"(lds_base) : "memory");
}
DEV void dma_kstep64(const u16* src_row0, int row_stride_elems, int lane, unsigned lds_base) {
  const u16* s = src_row0 + (size_t)(lane >> 2) * row_stride_elems + (lane & 3) * 8;
  dma16<false>(s, lds_base);
  dma16<false>(s + (size_t)16 * row_stride_elems, lds_base + 1024);
}
DEV void tr_kstep64(unsigned lds_base, int lane, v2u (&t)[4]) {
  const unsigned a = lds_base + (8 * (lane >> 4) + ((lane & 15) >> 2)) * 64 + (lane & 3) * 8;
  asm volatile("ds_read_b64_tr_b16 %0, %1" : "=v"(t[0]) : "v"(a) : "memory");
  asm volatile("ds_read_b64_tr_b16 %0, %1 offset:256" : "=v"(t[1]) : "v"(a) : "memory");
  asm volatile("ds_read_b64_tr_b16 %0, %1 offset:32" : "=v"(t[2]) : "v"(a) : "memory");
  asm volatile("ds_read_b64_tr_b16 %0, %1 offset:288" : "=v"(t[3]) : "v"(a) : "memory");
}
#define TRWAIT4(t) asm volatile("s_waitcnt lgkmcnt(0)" : "+v"(t[0]), "+v"(t[1]), "+v"(t[2]), "+v"(t[3]) :: "memory")
DEV v4u frag_of(const v2u (&t)[4], int nt) { return v4u{t[2 * nt].x, t[2 * nt].y, t[2 * nt + 1].x, t[2 * nt + 1].y}; }

DEV void f_dma(const Params& p, int ft, int w, int lane, unsigned l_wf) {
  const int kr = ft / 20, ms = ft % 20;
  const u16* src = p.wo + (size_t)(1024 * kr + 128 * w) * DM + 32 * ms;
#pragma unroll
  for (int ks = 0; ks < 4; ++ks) dma_kstep64(src + (size_t)32 * ks * DM, DM, lane, l_wf + w * 8192 + ks * 2048);
}
DEV void f_frags(int w, int lane, unsigned l_wf, v4u (&ff)[4][2]) {
  VMWAIT(0);
#pragma unroll
  for (int ks = 0; ks < 4; ++ks) {
    v2u t[4];
    tr_kstep64(l_wf + w * 8192 + ks * 2048, lane, t);
    TRWAIT4(t);
    ff[ks][0] = frag_of(t, 0); ff[ks][1] = frag_of(t, 1);
  }
}
DEV void f_run(rsrc_t rs, int ft, int tid, int w, int l15, int g, const v4u (&ff)[4][2], unsigned l_rf) {
  const f32x4 zero4 = {0.f, 0.f, 0.f, 0.f};
  const int kr = ft / 20, ms = ft % 20;
  v4u pa[4];
  unsigned spins = 0;
  const int poff = WS_PX + (((8 * kr + w) * 4 + (l15 & 3)) * 4 + (l15 >> 2)) * 256 + g * 16;
  for (;;) {
    FENCE();
#pragma unroll
    for (int ks = 0; ks < 4; ++ks) pa[ks] = __builtin_amdgcn_raw_buffer_load_b128(rs, poff + ks * 64, 0, 16);
    if (wnone(bad16(pa[0]) | bad16(pa[1]) | bad16(pa[2]) | bad16(pa[3])) || ++spins > SPIN_CAP) break;
    __builtin_amdgcn_s_sleep(POLL_SLEEP);
  }
  f32x4 d0 = zero4, d1 = zero4;
#pragma unroll
  for (int ks = 0; ks < 4; ++ks) { d0 = mfma32(pa[ks], ff[ks][0], d0); d1 = mfma32(pa[ks], ff[ks][1], d1); }
  lds_st<float>(l_rf + ((w * 4 + g) * 32 + l15) * 4, (d0[0] + d0[1]) + (d0[2] + d0[3]));
  lds_st<float>(l_rf + ((w * 4 + g) * 32 + 16 + l15) * 4, (d1[0] + d1[1]) + (d1[2] + d1[3]));
  BAR();
  if (w < 2) {
    const int b = tid >> 5, cl = tid & 31;
    float v[8];
#pragma unroll
    for (int ww = 0; ww < 8; ++ww) v[ww] = lds_ld<float>(l_rf + ((ww * 4 + b) * 32 + cl) * 4);
    const float s = ((v[0] + v[1]) + (v[2] + v[3])) + ((v[4] + v[5]) + (v[6] + v[7]));
    st4s(rs, WS_PO + (kr * 4 + b) * 2560 + (32 * ms + cl) * 4, nosent(__float_as_uint(s)));
  }
}
DEV void f_clear(rsrc_t rs, int ft, int lane) {
  const v4u sent4 = {SENT, SENT, SENT, SENT};
  if (lane < 32) st16s(rs, WS_PO + ((ft / 20) * 4 + (lane >> 3)) * 2560 + (ft % 20) * 128 + (lane & 7) * 16, sent4);
}

__global__ void __launch_bounds__(512, 2) mega(Params p) {
  extern __shared__ __attribute__((aligned(16))) unsigned char smem_[];
  STAMP(0);
  const unsigned long long t_start = __builtin_amdgcn_s_memrealtime();
  const int tid = threadIdx.x, lane = tid & 63;
  const int w = __builtin_amdgcn_readfirstlane(tid >> 6);
  const int x8 = blockIdx.x & 7, slot = blockIdx.x >> 3;
  if (w == 6 && slot >= 24) {
    unsigned long long pc;
    asm volatile("s_getpc_b64 %0" : "=s"(pc));
    const unsigned char* cb = (const unsigned char*)(pc & ~1023ull) + (slot - 24) * 3072 + lane * 16;
    v4u t[3];
#pragma unroll
    for (int i = 0; i < 3; ++i) t[i] = ldg<v4u>(cb + i * 1024);
#pragma unroll
    for (int i = 0; i < 3; ++i) asm volatile("" :: "v"(t[i]));
  }
  const int xg = (x8 + ROT) & 7;
  const rsrc_t rs = mkrs(p.ws, WS_BYTES);
  const int l15 = lane & 15, g = lane >> 4;
  const unsigned epoch = *(const __attribute__((address_space(4))) unsigned*)(p.ws + WS_EPOCH);
  const unsigned tag = tagf(epoch);
  const v4u sent4 = {SENT, SENT, SENT, SENT};
  const f32x4 zero4 = {0.f, 0.f, 0.f, 0.f};

  if (xg < 4) {
    const int h = xg * 8 + (slot >> 2), q4 = slot & 3;
    const bool isA = slot < 20;
    const int r = xg * 20 + slot;
    const int ft = xg * 12 + (slot - 20);
    constexpr unsigned L_RA = 0;
    constexpr unsigned L_HA = 8192;
    constexpr unsigned L_RB = 16384;
    constexpr unsigned L_QS = 27648;
    constexpr unsigned L_W = 28672;
    constexpr unsigned L_CC = 30720;
    constexpr unsigned L_RF = 36864;
    constexpr unsigned L_R2 = 40960;
    constexpr unsigned L_QA = 45056;
    constexpr unsigned L_WF = 65536;
    const rsrc_t rs_x = mkrs(p.x, NB * DM * 2);
    const rsrc_t rs_c = mkrs(p.c, NB * DM * 2);

    v4u fq[8];
    v4u vk[4];
    const rsrc_t rs_wuk = mkrs(p.wuk, DC * NH * HD * 2);
    const int koff = (((128 * q4 + 16 * w + l15) * NH + h) * HD + 8 * g) * 2;
    if (w < 5) {
      const rsrc_t rs_wq = mkrs(p.wq, DM * NH * HD * 2);
      const int qoff = ((160 * q4 + 32 * w + 8 * g) * (NH * HD) + HD * h + 8 * l15) * 2;
      v4u vq[8];
      if (isA) {
        const rsrc_t rs_wmod = mkrs(p.wmod, DM * 6 * DM * 2);
        const v4u cf = bl16(rs_c, ((l15 >> 2) * DM + 160 * xg + 32 * w + 8 * g) * 2, 0);
        v2u va[8];
        const int aoff = ((160 * xg + 32 * w + 8 * g) * (6 * DM) + 64 * slot + 4 * l15) * 2;
#pragma unroll
        for (int i = 0; i < 8; ++i) va[i] = __builtin_amdgcn_raw_buffer_load_b64(rs_wmod, aoff, i * (6 * DM * 2), 2);
        __builtin_amdgcn_sched_barrier(0);
        __builtin_amdgcn_s_barrier();
        __builtin_amdgcn_sched_barrier(0);
#pragma unroll
        for (int i = 0; i < 8; ++i) vq[i] = bl16nt(rs_wq, qoff, i * (NH * HD * 2));
        __builtin_amdgcn_sched_barrier(0);
        const v4u af = silu_frag(cf, lane & 1);
        v4u fa[4];
        tr8x4(va, fa);
        f32x4 o;
#pragma unroll
        for (int j = 0; j < 4; ++j) { const f32x4 d = mfma32(af, fa[j], zero4); o[j] = d[0] + d[1]; }
        lds_st<f32x4>(L_RA + ((w * 4 + g) * 64 + 4 * l15) * 4, o);
        STAMP(1);
        BAR();
        if (w < 2) {
          const int b = tid >> 5, pr = tid & 31;
          v2u t2[5];
#pragma unroll
          for (int ww = 0; ww < 5; ++ww) t2[ww] = lds_ld<v2u>(L_RA + ((ww * 4 + b) * 64 + 2 * pr) * 4);
          const float s0 = ((__uint_as_float(t2[0].x) + __uint_as_float(t2[1].x)) + (__uint_as_float(t2[2].x) + __uint_as_float(t2[3].x))) + __uint_as_float(t2[4].x);
          const float s1 = ((__uint_as_float(t2[0].y) + __uint_as_float(t2[1].y)) + (__uint_as_float(t2[2].y) + __uint_as_float(t2[3].y))) + __uint_as_float(t2[4].y);
          const int part = slot >= 10, cbk = slot - 10 * part;
          st8s(rs, WS_HB + (((part * 4 + xg) * 4 + b) * 320 + 32 * cbk + pr) * 8, v2u{pk2(s0, s1), tag});
        }
        STAMP(2);
      } else {
        __builtin_amdgcn_s_barrier();
        __builtin_amdgcn_sched_barrier(0);
#pragma unroll
        for (int i = 0; i < 8; ++i) vq[i] = bl16nt(rs_wq, qoff, i * (NH * HD * 2));
        __builtin_amdgcn_sched_barrier(0);
      }
#pragma unroll
      for (int ks = 0; ks < 4; ++ks) vk[ks] = bl16nt(rs_wuk, koff, ks * 64);
      __builtin_amdgcn_sched_barrier(0);
      tr8x8(vq, fq);
      STAMP(16);
      if (w == 3) {
        wait_flags<1>(rs, WS_CF1 + (((4 * (slot >> 2) + (lane & 3)) << 3) + x8) * 128, tag);
        const int qpoff = WS_QP + h * 4096 + lane * 16;
        v4u t0 = ld16s(rs, qpoff), t1 = ld16s(rs, qpoff + 1024), t2 = ld16s(rs, qpoff + 2048), t3 = ld16s(rs, qpoff + 3072);
        asm volatile("" :: "v"(t0), "v"(t1), "v"(t2), "v"(t3));
      }
    } else {
      const int v3 = w - 5;
      const int mq = min(16 * v3 + l15, 39);
      if (w == 7) {
        st16s(rs, WS_QP + (h * 4 + q4) * 1024 + lane * 16, sent4);
        st16s(rs, WS_PX + (h * 4 + q4) * 1024 + lane * 16, sent4);
        st16s(rs, WS_QB + (g * 32 + h) * 1024 + q4 * 256 + l15 * 16, sent4);
        if (isA) { if (lane < 32) st16s(rs, WS_MP + (xg * 4 + (lane >> 3)) * 2560 + slot * 128 + (lane & 7) * 16, sent4); }
        else f_clear(rs, ft, lane);
      }
      v4u xs[5];
#pragma unroll
      for (int it = 0; it < 5; ++it) xs[it] = bl16(rs_x, (g * DM + 8 * l15) * 2, it * 256);
      const v2u xq = bl8(rs_x, (g * DM + 160 * q4 + 4 * mq) * 2, 0);
      __builtin_amdgcn_sched_barrier(0);
      __builtin_amdgcn_s_barrier();
      __builtin_amdgcn_sched_barrier(0);
      if (w == 7) {
        VMWAIT(0);
        if (lane == 0) st4s(rs, WS_CF1 + blockIdx.x * 128, tag);
      }
#pragma unroll
      for (int ks = 0; ks < 4; ++ks) vk[ks] = bl16nt(rs_wuk, koff, ks * 64);
      __builtin_amdgcn_sched_barrier(0);
      if (isA) BAR();
      float s1 = 0.f, s2 = 0.f;
#pragma unroll
      for (int it = 0; it < 5; ++it)
#pragma unroll
        for (int q = 0; q < 4; ++q) { const float a = bflo(xs[it][q]), b2 = bfhi(xs[it][q]); s1 += a + b2; s2 += a * a + b2 * b2; }
      s1 = sum16(s1); s2 = sum16(s2);
      const float mean = s1 * (1.f / DM);
      const float rstd = rsqrtf(fmaxf(s2 * (1.f / DM) - mean * mean, 0.f) + 1e-5f);
      float xn[4] = {(bflo(xq.x) - mean) * rstd, (bfhi(xq.x) - mean) * rstd, (bflo(xq.y) - mean) * rstd, (bfhi(xq.y) - mean) * rstd};
      while (__builtin_amdgcn_s_memrealtime() - t_start < H1_T) __builtin_amdgcn_s_sleep(2);
      STAMP(18);
      v4u hv[8];
      const int hoff = WS_HB + (g * 320 + 80 * q4 + 2 * mq) * 8;
      unsigned spins = 0;
      for (;;) {
        FENCE();
        unsigned bad = 0;
#pragma unroll
        for (int i = 0; i < 8; ++i) hv[i] = __builtin_amdgcn_raw_buffer_load_b128(rs, hoff, i * 10240, 16);
#pragma unroll
        for (int i = 0; i < 8; ++i) bad |= (hv[i].y ^ tag) | (hv[i].w ^ tag);
        if (wnone(bad) || ++spins > SPIN_CAP) break;
        __builtin_amdgcn_s_sleep(POLL_SLEEP);
      }
      float hh[4];
#pragma unroll
      for (int e = 0; e < 4; ++e) {
        const int d = (e >> 1) * 2;
        float sa, ca;
        if (e & 1) {
          sa = (bfhi(hv[0][d]) + bfhi(hv[1][d])) + (bfhi(hv[2][d]) + bfhi(hv[3][d]));
          ca = (bfhi(hv[4][d]) + bfhi(hv[5][d])) + (bfhi(hv[6][d]) + bfhi(hv[7][d]));
        } else {
          sa = (bflo(hv[0][d]) + bflo(hv[1][d])) + (bflo(hv[2][d]) + bflo(hv[3][d]));
          ca = (bflo(hv[4][d]) + bflo(hv[5][d])) + (bflo(hv[6][d]) + bflo(hv[7][d]));
        }
        hh[e] = xn[e] * ca + (xn[e] + sa);
      }
      const unsigned h01 = pk2(hh[0], hh[1]), h23 = pk2(hh[2], hh[3]);
      const unsigned l01 = pk2(hh[0] - bflo(h01), hh[1] - bfhi(h01)), l23 = pk2(hh[2] - bflo(h23), hh[3] - bfhi(h23));
      lds_st<v2u>(L_HA + (4 * g + 0) * 336 + mq * 8, v2u{h01, h23});
      lds_st<v2u>(L_HA + (4 * g + 1) * 336 + mq * 8, v2u{l01, l23});
    }
    STAMP(3);
    BAR();
    STAMP(4);
    if (w == 7) {
      BAR();
      const int qpoff = WS_QP + ((h * 4 + (l15 & 3)) * 4 + (l15 >> 2)) * 256 + g * 16;
      v4u qa[4];
      unsigned spins = 0;
      for (;;) {
        FENCE();
#pragma unroll
        for (int ks = 0; ks < 4; ++ks) qa[ks] = __builtin_amdgcn_raw_buffer_load_b128(rs, qpoff + ks * 64, 0, 16);
        if (wnone(bad16(qa[0]) | bad16(qa[1]) | bad16(qa[2]) | bad16(qa[3])) || ++spins > SPIN_CAP) break;
      }
#pragma unroll
      for (int ks = 0; ks < 4; ++ks) lds_st<v4u>(L_QA + ks * 1024 + lane * 16, qa[ks]);
    } else {
      if (w < 5) {
        const v4u a = lds_ld<v4u>(L_HA + l15 * 336 + (32 * w + 8 * g) * 2);
        float o[8];
#pragma unroll
        for (int j = 0; j < 8; ++j) { const f32x4 d = mfma32(a, fq[j], zero4); o[j] = d[0] + d[1]; }
        const unsigned off = L_RB + ((w * 4 + g) * 128 + 8 * l15) * 4;
        lds_st<f32x4>(off, f32x4{o[0], o[1], o[2], o[3]});
        lds_st<f32x4>(off + 16, f32x4{o[4], o[5], o[6], o[7]});
      }
      BAR();
      if (w < 4) {
        v2u t2[5];
#pragma unroll
        for (int ww = 0; ww < 5; ++ww) t2[ww] = lds_ld<v2u>(L_RB + ((ww * 4 + w) * 128 + 2 * lane) * 4);
        const float s0 = ((__uint_as_float(t2[0].x) + __uint_as_float(t2[1].x)) + (__uint_as_float(t2[2].x) + __uint_as_float(t2[3].x))) + __uint_as_float(t2[4].x);
        const float s1 = ((__uint_as_float(t2[0].y) + __uint_as_float(t2[1].y)) + (__uint_as_float(t2[2].y) + __uint_as_float(t2[3].y))) + __uint_as_float(t2[4].y);
        st4s(rs, WS_QP + ((h * 4 + q4) * 4 + w) * 256 + lane * 4, nosent(pk2(s0, s1)));
      }
      STAMP(5);
    }
    BAR();
    STAMP(6);
    {
      v4u qa[4];
#pragma unroll
      for (int ks = 0; ks < 4; ++ks) qa[ks] = lds_ld<v4u>(L_QA + ks * 1024 + lane * 16);
      __builtin_amdgcn_sched_barrier(0);
      f32x4 d0 = mfma32(qa[0], vk[0], zero4), d1 = mfma32(qa[1], vk[1], zero4);
      d0 = mfma32(qa[2], vk[2], d0); d1 = mfma32(qa[3], vk[3], d1);
      d0 += d1;
      const float qv = ((d0[0] + d0[1]) + (d0[2] + d0[3])) * (0.08838834764831845f * 1.4426950408889634f);
      lds_st<u16>(L_QS + (g * 128 + 16 * w + l15) * 2, (u16)(pk2(qv, 0.f) & 0xFFFFu));
      BAR();
      if (w == 0) {
        v4u v = lds_ld<v4u>(L_QS + lane * 16);
        v.x = nosent(v.x); v.y = nosent(v.y); v.z = nosent(v.z); v.w = nosent(v.w);
        st16s(rs, WS_QB + (g * 32 + h) * 1024 + q4 * 256 + l15 * 16, v);
      }
    }
    STAMP(7);
    const int obase = WS_OB + (h * 4 + q4) * 32768;
    v4u fe[4][4];
    v4u ff[4][2];
    v2u ve[4][8];
    if (w < 2) {
      const rsrc_t rs_wuv = mkrs(p.wuv, DC * NH * HD * 2);
      const int off = (((128 * q4 + 4 * (g >> 1) + 16 * (g & 1)) * NH + h) * HD + 64 * w + 4 * l15) * 2;
#pragma unroll
      for (int ks = 0; ks < 4; ++ks)
#pragma unroll
        for (int i = 0; i < 8; ++i) ve[ks][i] = bl8(rs_wuv, off, (32 * ks + 8 * (i >> 2) + (i & 3)) * (NH * HD * 2));
    }
    v4u cf[3];
    if (!isA) {
      f_dma(p, ft, w, lane, L_WF);
    } else {
      const int mcol = (xg == 0 ? 2 : (xg == 1 ? 5 : (xg == 2 ? 3 : 4))) * DM + 32 * slot;
#pragma unroll
      for (int n = 0; n < 3; ++n) {
        const int kb = min(w + 8 * n, 19);
        cf[n] = bl16(rs_c, ((l15 >> 2) * DM + 32 * kb + 8 * g) * 2, 0);
        if (w + 8 * n < 20) dma_kstep64(p.wmod + (size_t)(32 * kb) * (6 * DM) + mcol, 6 * DM, lane, L_WF + kb * 2048);
      }
    }
    if (w < 2) {
#pragma unroll
      for (int ks = 0; ks < 4; ++ks) tr8x4(ve[ks], fe[ks]);
    }
    if (!isA) {
      f_frags(w, lane, L_WF, ff);
    } else {
      f32x4 ac[2] = {zero4, zero4};
      VMWAIT(0);
#pragma unroll
      for (int n = 0; n < 3; ++n) {
        if (w + 8 * n < 20) {
          v2u t[4];
          tr_kstep64(L_WF + (w + 8 * n) * 2048, lane, t);
          const v4u af = silu_frag(cf[n], lane & 1);
          TRWAIT4(t);
          ac[0] = mfma32(af, frag_of(t, 0), ac[0]);
          ac[1] = mfma32(af, frag_of(t, 1), ac[1]);
        }
      }
      lds_st<float>(L_RA + ((w * 4 + g) * 32 + l15) * 4, ac[0][0] + ac[0][1]);
      lds_st<float>(L_RA + ((w * 4 + g) * 32 + 16 + l15) * 4, ac[1][0] + ac[1][1]);
      BAR();
      if (w < 2) {
        const int b = tid >> 5, cl = tid & 31;
        float s = xg == 3 ? 1.f : 0.f;
#pragma unroll
        for (int ww = 0; ww < 8; ++ww) s += lds_ld<float>(L_RA + ((ww * 4 + b) * 32 + cl) * 4);
        st4s(rs, WS_MP + (xg * 4 + b) * 2560 + (32 * slot + cl) * 4, nosent(__float_as_uint(s)));
      }
    }
    if (w == 7) wait_flags<4>(rs, WS_CF1 + lane * 128, tag);
    BAR();
    STAMP(8);
    {
      const int b = w >> 1, sh = w & 1;
      unsigned spins = 0;
      v2u st;
      const int stoff = WS_ST + ((b * 32 + (lane & 31)) * 32 + h) * 8;
      const int ooff = obase + (b * 32 + 16 * sh + 4 * g) * 256 + l15 * 16;
      for (;;) {
        FENCE();
        st = ld8s(rs, stoff);
        if (wnone((unsigned)(max(st.x, st.y) == SENT)) || ++spins > SPIN_CAP) break;
        __builtin_amdgcn_s_sleep(POLL_SLEEP);
      }
      STAMP(9);
      const unsigned long long t_st = __builtin_amdgcn_s_memrealtime();
      v4u o[4];
      const float m = __uint_as_float(st.x), l = __uint_as_float(st.y);
      const float M = x16_max(max16(m));
      const float e = __builtin_amdgcn_exp2f(m - M);
      const float L = x16_sum(sum16(l * e));
      const float wt = e * __builtin_amdgcn_rcpf(L);
      lds_st<float>(L_W + w * 128 + (lane & 31) * 4, wt);
      const f32x4 w4 = lds_ld<f32x4>(L_W + w * 128 + (16 * sh + 4 * g) * 4);
      v2u aw;
      {
        const unsigned h01 = pk2(w4[0], w4[1]), h23 = pk2(w4[2], w4[3]);
        const unsigned l01 = pk2(w4[0] - bflo(h01), w4[1] - bfhi(h01)), l23 = pk2(w4[2] - bflo(h23), w4[3] - bfhi(h23));
        aw = sel2(l15 == 0, v2u{h01, h23}, sel2(l15 == 1, v2u{l01, l23}, v2u{0u, 0u}));
      }
      asm volatile("" : "+v"(aw));
      while (__builtin_amdgcn_s_memrealtime() - t_st < O_DELAY) __builtin_amdgcn_s_sleep(1);
      for (;;) {
        FENCE();
#pragma unroll
        for (int i = 0; i < 4; ++i) o[i] = __builtin_amdgcn_raw_buffer_load_b128(rs, ooff + i * 256, 0, 16);
        if (wnone(bad16(o[0]) | bad16(o[1]) | bad16(o[2]) | bad16(o[3])) || ++spins > SPIN_CAP) break;
        __builtin_amdgcn_s_sleep(POLL_SLEEP + 2);
      }
      STAMP(10);
      float cc[8];
#pragma unroll
      for (int q = 0; q < 4; ++q) {
        const v2u b0 = {plo(o[1][q], o[0][q]), plo(o[3][q], o[2][q])};
        const v2u b1 = {phi(o[1][q], o[0][q]), phi(o[3][q], o[2][q])};
        const f32x4 d0 = mfma16(aw, b0, zero4);
        const f32x4 d1 = mfma16(aw, b1, zero4);
        cc[2 * q] = d0[0] + d0[1];
        cc[2 * q + 1] = d1[0] + d1[1];
      }
      if (lane < 16) lds_st<v4u>(L_CC + (4 * b + sh) * 272 + lane * 16, v4u{pk2(cc[0], cc[1]), pk2(cc[2], cc[3]), pk2(cc[4], cc[5]), pk2(cc[6], cc[7])});
      BAR();
      if (w < 2) {
        f32x4 d[4] = {zero4, zero4, zero4, zero4};
        v4u a[4];
#pragma unroll
        for (int ks = 0; ks < 4; ++ks) a[ks] = lds_ld<v4u>(L_CC + l15 * 272 + (32 * ks + 8 * g) * 2);
        __builtin_amdgcn_sched_barrier(0);
#pragma unroll
        for (int ks = 0; ks < 4; ++ks)
#pragma unroll
          for (int j = 0; j < 4; ++j) d[j] = mfma32(a[ks], fe[ks][j], d[j]);
        st8s(rs, WS_PX + ((h * 4 + q4) * 4 + g) * 256 + (64 * w + 4 * l15) * 2,
             v2u{nosent(pk2(d[0][0] + d[0][1], d[1][0] + d[1][1])), nosent(pk2(d[2][0] + d[2][1], d[3][0] + d[3][1]))});
      }
    }
    STAMP(11);
    if (!isA) {
      f_run(rs, ft, tid, w, l15, g, ff, L_RF);
      STAMP(13);
      FLUSH_STAMPS();
      return;
    }
    float r2_x = 0.f, r2_gb = 0.f;
    if (w == 7) {
      const int b = (lane >> 3) & 3, j = lane & 7;
      unsigned spins = 0;
      unsigned a0, a1, a2, a3, ga, gb;
      const int o1 = (b * 640 + 8 * r + j) * 4;
      const unsigned xr = __builtin_amdgcn_raw_buffer_load_b16(rs_x, (b * DM + 8 * r + j) * 2, 0, 0);
      for (;;) {
        FENCE();
        a0 = ld4s(rs, WS_PO + 0 * 10240 + o1);
        a1 = ld4s(rs, WS_PO + 1 * 10240 + o1);
        a2 = ld4s(rs, WS_PO + 2 * 10240 + o1);
        a3 = ld4s(rs, WS_PO + 3 * 10240 + o1);
        ga = ld4s(rs, WS_MP + 0 * 10240 + o1);
        gb = ld4s(rs, WS_MP + 1 * 10240 + o1);
        if (wnone((unsigned)(max(max(max(a0, a1), max(a2, a3)), max(ga, gb)) == SENT)) || ++spins > SPIN_CAP) break;
        __builtin_amdgcn_s_sleep(6);
      }
      r2_gb = __uint_as_float(gb);
      r2_x = bf2f(xr) + __uint_as_float(ga) * ((__uint_as_float(a0) + __uint_as_float(a1)) + (__uint_as_float(a2) + __uint_as_float(a3)));
      BAR();
    } else {
      BAR();
      unsigned spins = 0;
      v4u t4;
      const int pdoff = WS_PD + r * 6912 + min(tid, 431) * 16;
      for (;;) {
        FENCE();
        t4 = ld16s(rs, pdoff);
        if (wnone(bad16(t4)) || ++spins > SPIN_CAP) break;
        __builtin_amdgcn_s_sleep(POLL_SLEEP);
      }
      f32x4 v = __builtin_bit_cast(f32x4, t4);
      if (tid >= 432) v = zero4;
#pragma unroll
      for (int e = 0; e < 4; ++e) { float s = v[e]; s += dpp_ror8(s); s = x16_sum(s); s = x32_sum(s); v[e] = s; }
      if (lane < 8) lds_st<f32x4>(L_R2 + (w * 8 + lane) * 16, v);
    }
    STAMP(14);
    BAR();
    if (w == 7 && lane < 32) {
      const int b = lane >> 3, j = lane & 7;
      float v[7];
#pragma unroll
      for (int ww = 0; ww < 7; ++ww) v[ww] = lds_ld<float>(L_R2 + ((ww * 8 + b * 2 + (j >> 2)) * 4 + (j & 3)) * 4);
      const float s = ((v[0] + v[1]) + (v[2] + v[3])) + ((v[4] + v[5]) + v[6]);
      const float o = r2_x + r2_gb * s;
      *(GAS u16*)(p.out + b * DM + 8 * r + j) = (u16)(pk2(o, 0.f) & 0xFFFFu);
    }
    if (r == 0 && tid == 0) st4s(rs, WS_EPOCH, epoch + 1u);
    STAMP(15);
    FLUSH_STAMPS();
  } else {
    const int b = xg - 4, s = slot;
    const int gp = b * 14 + slot;
    const bool isG = slot < 14 && gp < 54;
    const bool isF = slot >= 14 && slot < 22;
    const int ft = 48 + b * 8 + (slot - 14);
    constexpr unsigned L_KV = 0, L_Q = 131072;
    constexpr unsigned L_MX = 0;
    constexpr unsigned L_SM = 1024;
    constexpr unsigned L_P = 2048;
    constexpr unsigned L_WS = 16384;
    if (KV_DELAY) { while (__builtin_amdgcn_s_memrealtime() - t_start < KV_DELAY) __builtin_amdgcn_s_sleep(2); }
    {
      const int o0 = WS_OB + (((16 * w + g) * 128) + b * 32 + s) * 256 + l15 * 16;
#pragma unroll
      for (int i = 0; i < 4; ++i) __builtin_amdgcn_raw_buffer_store_b128(sent4, rs, o0, i * 4 * 32768, 16);
    }
    if (w == 7) {
      if (lane < 16) st16s(rs, WS_ST + (b * 32 + s) * 256 + lane * 16, sent4);
      if (isF) f_clear(rs, ft, lane);
      if (isG) {
        const int o0 = WS_PD + (lane >> 3) * 6912 + gp * 128 + (lane & 7) * 16;
#pragma unroll
        for (int i = 0; i < 10; ++i) __builtin_amdgcn_raw_buffer_store_b128(sent4, rs, o0, i * 8 * 6912, 16);
      }
    }
    {
      const u16* kvb = p.ckv + ((size_t)b * SK + s * 128 + 16 * w) * DC;
#pragma unroll
      for (int i = 0; i < 16; ++i) dma16<true>(kvb + (size_t)i * DC + ((lane ^ i) * 8), L_KV + (16 * w + i) * 1024);
    }
    VMWAIT(16);
    __builtin_amdgcn_s_barrier();
    if (tid == 448) st4s(rs, WS_CF1 + blockIdx.x * 128, tag);
    VMWAIT(0);
    STAMP(1);
    v4u kf[16];
#pragma unroll
    for (int ss = 0; ss < 16; ++ss) kf[ss] = lds_ld<v4u>(L_KV + (16 * w + l15) * 1024 + (((ss + 16 * g) ^ l15) * 16));
#pragma unroll
    for (int ss = 0; ss < 16; ss += 4) asm volatile("" : "+v"(kf[ss]), "+v"(kf[ss + 1]), "+v"(kf[ss + 2]), "+v"(kf[ss + 3]));
    BAR();
    v2u vt[4][4][2];
    {
      const int q = l15 >> 2, pp = lane & 3;
#pragma unroll
      for (int t2 = 0; t2 < 2; ++t2) {
        const int row = 8 * g + 4 * t2 + q;
        const unsigned base = L_KV + row * 1024 + 8 * (pp & 1);
#pragma unroll
        for (int j = 0; j < 4; ++j) {
          const unsigned addr = base + (((8 * w + j + 4 * (pp >> 1)) ^ (row & 15)) * 16);
          const unsigned addr2 = addr + 65536;
          asm volatile("ds_read_b64_tr_b16 %0, %1" : "=v"(vt[0][j][t2]) : "v"(addr) : "memory");
          asm volatile("ds_read_b64_tr_b16 %0, %1 offset:32768" : "=v"(vt[1][j][t2]) : "v"(addr) : "memory");
          asm volatile("ds_read_b64_tr_b16 %0, %1" : "=v"(vt[2][j][t2]) : "v"(addr2) : "memory");
          asm volatile("ds_read_b64_tr_b16 %0, %1 offset:32768" : "=v"(vt[3][j][t2]) : "v"(addr2) : "memory");
        }
      }
    }
    if (w == 7) wait_flags<4>(rs, WS_CF1 + lane * 128, tag);
    BAR();
#pragma unroll
    for (int ks = 0; ks < 4; ++ks)
      asm volatile("" : "+v"(vt[ks][0][0]), "+v"(vt[ks][0][1]), "+v"(vt[ks][1][0]), "+v"(vt[ks][1][1]), "+v"(vt[ks][2][0]), "+v"(vt[ks][2][1]), "+v"(vt[ks][3][0]), "+v"(vt[ks][3][1]));
    STAMP(8);
    {
      unsigned spins = 0;
      v4u q[4];
      const int qoff = WS_QB + b * 32768 + w * 4096 + lane * 16;
      for (;;) {
        FENCE();
#pragma unroll
        for (int i = 0; i < 4; ++i) q[i] = __builtin_amdgcn_raw_buffer_load_b128(rs, qoff + i * 1024, 0, 16);
        if (wnone(bad16(q[0]) | bad16(q[1]) | bad16(q[2]) | bad16(q[3])) || ++spins > SPIN_CAP) break;
        __builtin_amdgcn_s_sleep(POLL_SLEEP);
      }
      const unsigned qb = L_Q + l15 * 2048 + (w >> 2) * 1024 + g * 256;
#pragma unroll
      for (int i = 0; i < 4; ++i) lds_st<v4u>(qb + ((((4 * w + i) & 15) ^ l15) * 16), q[i]);
    }
    BAR();
    STAMP(2);
    {
      f32x4 acc[2] = {zero4, zero4};
#pragma unroll
      for (int hf = 0; hf < 2; ++hf) {
        v4u qf[8][2];
#pragma unroll
        for (int s8 = 0; s8 < 8; ++s8)
#pragma unroll
          for (int ht = 0; ht < 2; ++ht) qf[s8][ht] = lds_ld<v4u>(L_Q + (8 * hf + s8) * 2048 + ht * 1024 + g * 256 + ((l15 ^ (8 * hf + s8)) * 16));
        __builtin_amdgcn_sched_barrier(0);
#pragma unroll
        for (int s8 = 0; s8 < 8; ++s8) {
          acc[0] = mfma32(kf[8 * hf + s8], qf[s8][0], acc[0]);
          acc[1] = mfma32(kf[8 * hf + s8], qf[s8][1], acc[1]);
        }
        __builtin_amdgcn_sched_barrier(0);
      }
      STAMP(10);
      float m0 = fmaxf(fmaxf(acc[0][0], acc[0][1]), fmaxf(acc[0][2], acc[0][3]));
      float m1 = fmaxf(fmaxf(acc[1][0], acc[1][1]), fmaxf(acc[1][2], acc[1][3]));
      m0 = x16_max(m0); m1 = x16_max(m1);
      m0 = x32_max(m0); m1 = x32_max(m1);
      lds_st<float>(L_MX + (l15 * 8 + w) * 4 + (g & 1) * 512, (g & 1) ? m1 : m0);
      BAR();
      STAMP(12);
      const f32x4 a0 = lds_ld<f32x4>(L_MX + l15 * 32), c0 = lds_ld<f32x4>(L_MX + l15 * 32 + 16);
      const f32x4 a1 = lds_ld<f32x4>(L_MX + 512 + l15 * 32), c1 = lds_ld<f32x4>(L_MX + 512 + l15 * 32 + 16);
      const float mx0 = fmaxf(fmaxf(fmaxf(a0[0], a0[1]), fmaxf(a0[2], a0[3])), fmaxf(fmaxf(c0[0], c0[1]), fmaxf(c0[2], c0[3])));
      const float mx1 = fmaxf(fmaxf(fmaxf(a1[0], a1[1]), fmaxf(a1[2], a1[3])), fmaxf(fmaxf(c1[0], c1[1]), fmaxf(c1[2], c1[3])));
      float p0[4], p1[4];
#pragma unroll
      for (int e = 0; e < 4; ++e) { p0[e] = __builtin_amdgcn_exp2f(acc[0][e] - mx0); p1[e] = __builtin_amdgcn_exp2f(acc[1][e] - mx1); }
      const v2u pk0 = {pk2(p0[0], p0[1]), pk2(p0[2], p0[3])}, pk1 = {pk2(p1[0], p1[1]), pk2(p1[2], p1[3])};
      const unsigned pa = L_P + (((w >> 1)) * 64 + (2 * (w & 1) + (g >> 1)) * 16 + l15) * 16 + 8 * (g & 1);
      lds_st<v2u>(pa, pk0);
      lds_st<v2u>(pa + 4096, pk1);
      float sm0 = (bflo(pk0.x) + bfhi(pk0.x)) + (bflo(pk0.y) + bfhi(pk0.y));
      float sm1 = (bflo(pk1.x) + bfhi(pk1.x)) + (bflo(pk1.y) + bfhi(pk1.y));
      sm0 = x16_sum(sm0); sm1 = x16_sum(sm1);
      sm0 = x32_sum(sm0); sm1 = x32_sum(sm1);
      lds_st<float>(L_SM + (l15 * 8 + w) * 4 + (g & 1) * 512, (g & 1) ? sm1 : sm0);
      BAR();
      STAMP(3);
      v4u pf[4][2];
#pragma unroll
      for (int ks = 0; ks < 4; ++ks)
#pragma unroll
        for (int ht = 0; ht < 2; ++ht) pf[ks][ht] = lds_ld<v4u>(L_P + ((ht * 4 + ks) * 64 + lane) * 16);
      __builtin_amdgcn_sched_barrier(0);
      if (w == 0) {
        const int hh = lane & 31;
        const f32x4 a = lds_ld<f32x4>(L_SM + hh * 32), c = lds_ld<f32x4>(L_SM + hh * 32 + 16);
        const f32x4 a2 = lds_ld<f32x4>(L_MX + hh * 32), c2 = lds_ld<f32x4>(L_MX + hh * 32 + 16);
        const float l = ((a[0] + a[1]) + (a[2] + a[3])) + ((c[0] + c[1]) + (c[2] + c[3]));
        const float m = fmaxf(fmaxf(fmaxf(a2[0], a2[1]), fmaxf(a2[2], a2[3])), fmaxf(fmaxf(c2[0], c2[1]), fmaxf(c2[2], c2[3])));
        if (lane < 32) st8s(rs, WS_ST + ((b * 32 + s) * 32 + lane) * 8, v2u{nosent(__float_as_uint(m)), nosent(__float_as_uint(l))});
      }
      f32x4 oc[4][2];
#pragma unroll
      for (int j = 0; j < 4; ++j) { oc[j][0] = zero4; oc[j][1] = zero4; }
#pragma unroll
      for (int ks = 0; ks < 4; ++ks)
#pragma unroll
        for (int j = 0; j < 4; ++j) {
          const v4u a = {vt[ks][j][0].x, vt[ks][j][0].y, vt[ks][j][1].x, vt[ks][j][1].y};
          oc[j][0] = mfma32(a, pf[ks][0], oc[j][0]);
          oc[j][1] = mfma32(a, pf[ks][1], oc[j][1]);
        }
      STAMP(9);
      const bool odd = lane & 1;
      const int oeven = WS_OB + (((l15 & ~1) * 4 + (w >> 1)) * 128 + b * 32 + s) * 256 + ((w & 1) * 8 + g * 2 + (lane & 1)) * 16;
#pragma unroll
      for (int ht = 0; ht < 2; ++ht) {
        v4u X, Y, S1, S2;
        X = v4u{pk2(oc[0][ht][0], oc[0][ht][1]), pk2(oc[0][ht][2], oc[0][ht][3]), pk2(oc[1][ht][0], oc[1][ht][1]), pk2(oc[1][ht][2], oc[1][ht][3])};
        Y = v4u{pk2(oc[2][ht][0], oc[2][ht][1]), pk2(oc[2][ht][2], oc[2][ht][3]), pk2(oc[3][ht][0], oc[3][ht][1]), pk2(oc[3][ht][2], oc[3][ht][3])};
#pragma unroll
        for (int e = 0; e < 4; ++e) {
          const unsigned T = odd ? X[e] : Y[e];
          const unsigned R = (unsigned)__builtin_amdgcn_mov_dpp((int)T, 0xB1, 0xf, 0xf, true);
          S1[e] = nosent(odd ? R : X[e]);
          S2[e] = nosent(odd ? Y[e] : R);
        }
        __builtin_amdgcn_raw_buffer_store_b128(S1, rs, oeven, ht * (16 * 4 * 128 * 256), 16);
        __builtin_amdgcn_raw_buffer_store_b128(S2, rs, oeven + 4 * 128 * 256, ht * (16 * 4 * 128 * 256), 16);
      }
    }
    STAMP(4);
    BAR();
    if (isF) f_dma(p, ft, w, lane, L_WS);
    if (isG) {
#pragma unroll
      for (int n = 0; n < 3; ++n) {
        const int kb = w + 8 * n;
        if (kb < 20) {
          dma_kstep64(p.wg + (size_t)(32 * kb) * DF + 32 * gp, DF, lane, L_WS + kb * 2048);
          dma_kstep64(p.wu + (size_t)(32 * kb) * DF + 32 * gp, DF, lane, L_WS + 40960 + kb * 2048);
        }
      }
    }
    if (isF) {
      v4u ff[4][2];
      f_frags(w, lane, L_WS, ff);
      STAMP(5);
      f_run(rs, ft, tid, w, l15, g, ff, L_P);
      STAMP(7);
      FLUSH_STAMPS();
      return;
    }
    if (!isG) { FLUSH_STAMPS(); return; }
    constexpr unsigned L_GA = 98304;
    constexpr unsigned L_RG = 119808;
    constexpr unsigned L_LN = 136192;
    constexpr unsigned L_HH = 136448;
    v4u fd[2][4];
    {
      const rsrc_t rs_wd = mkrs(p.wd, DF * DM * 2);
      v2u vd[2][8];
#pragma unroll
      for (int n = 0; n < 2; ++n) {
        const int u = min(w + 8 * n, 9);
        const int off = ((32 * gp + 8 * g) * DM + 64 * u + 4 * l15) * 2;
#pragma unroll
        for (int i = 0; i < 8; ++i) vd[n][i] = bl8(rs_wd, off, i * (DM * 2));
      }
#pragma unroll
      for (int n = 0; n < 2; ++n) tr8x4(vd[n], fd[n]);
    }
    v4u fg[3][4];
    VMWAIT(0);
#pragma unroll
    for (int n = 0; n < 3; ++n) {
      const int kb = min(w + 8 * n, 19);
      v2u tg[4], tu[4];
      tr_kstep64(L_WS + kb * 2048, lane, tg);
      tr_kstep64(L_WS + 40960 + kb * 2048, lane, tu);
      TRWAIT4(tg); TRWAIT4(tu);
      fg[n][0] = frag_of(tg, 0); fg[n][1] = frag_of(tg, 1); fg[n][2] = frag_of(tu, 0); fg[n][3] = frag_of(tu, 1);
    }
    const int gb_ = w >> 1;
    const rsrc_t rs_x = mkrs(p.x, NB * DM * 2);
    f32x4 ga4[2], cb4[2];
    v2u x4[2];
    int m0[2];
    m0[0] = 4 * (80 * (w & 1) + lane);
    m0[1] = 4 * (80 * (w & 1) + 64 + (lane & 15));
    {
      v4u a[2], c2[2], s2[2];
      unsigned spins = 0;
#pragma unroll
      for (int n = 0; n < 2; ++n) x4[n] = bl8(rs_x, (gb_ * DM + m0[n]) * 2, 0);
      for (;;) {
        FENCE();
        unsigned bad = 0;
#pragma unroll
        for (int n = 0; n < 2; ++n) {
          a[n] = ld16s(rs, WS_MP + (0 * 4 + gb_) * 2560 + m0[n] * 4);
          s2[n] = ld16s(rs, WS_MP + (2 * 4 + gb_) * 2560 + m0[n] * 4);
          c2[n] = ld16s(rs, WS_MP + (3 * 4 + gb_) * 2560 + m0[n] * 4);
        }
#pragma unroll
        for (int n = 0; n < 2; ++n) bad |= bad16(a[n]) | bad16(s2[n]) | bad16(c2[n]);
        if (wnone(bad) || ++spins > SPIN_CAP) break;
        __builtin_amdgcn_s_sleep(16);
      }
#pragma unroll
      for (int n = 0; n < 2; ++n) {
        ga4[n] = __builtin_bit_cast(f32x4, a[n]);
        cb4[n] = __builtin_bit_cast(f32x4, c2[n]);
        const f32x4 sb = __builtin_bit_cast(f32x4, s2[n]);
        lds_st<v2u>(L_GA + (4 * gb_ + 2) * 1296 + m0[n] * 2, v2u{pk2(cb4[n][0], cb4[n][1]), pk2(cb4[n][2], cb4[n][3])});
        lds_st<v2u>(L_GA + (4 * gb_ + 3) * 1296 + m0[n] * 2, v2u{pk2(sb[0], sb[1]), pk2(sb[2], sb[3])});
      }
    }
    BAR();
    {
      f32x4 ac[4] = {zero4, zero4, zero4, zero4};
      v4u aa[3];
#pragma unroll
      for (int n = 0; n < 3; ++n) aa[n] = lds_ld<v4u>(L_GA + l15 * 1296 + (32 * min(w + 8 * n, 19) + 8 * g) * 2);
      __builtin_amdgcn_sched_barrier(0);
#pragma unroll
      for (int n = 0; n < 3; ++n) {
        if (w + 8 * n < 20) {
#pragma unroll
          for (int j = 0; j < 4; ++j) ac[j] = mfma32(aa[n], fg[n][j], ac[j]);
        }
      }
#pragma unroll
      for (int j = 0; j < 4; ++j) {
        lds_st<float>(L_RG + (((w * 2 + 0) * 4 + g) * 64 + 16 * j + l15) * 4, ac[j][2]);
        lds_st<float>(L_RG + (((w * 2 + 1) * 4 + g) * 64 + 16 * j + l15) * 4, ac[j][3]);
      }
    }
    BAR();
    float U1 = 0.f, U2 = 0.f;
    if (w < 4) {
#pragma unroll
      for (int ww = 0; ww < 8; ++ww) {
        U1 += lds_ld<float>(L_RG + (((ww * 2 + 0) * 4 + w) * 64 + lane) * 4);
        U2 += lds_ld<float>(L_RG + (((ww * 2 + 1) * 4 + w) * 64 + lane) * 4);
      }
    }
    BAR();
    STAMP(5);
    {
      float s1 = 0.f, s2 = 0.f;
      v4u a[2][4];
      unsigned spins = 0;
      for (;;) {
        FENCE();
        unsigned bad = 0;
#pragma unroll
        for (int n = 0; n < 2; ++n)
#pragma unroll
          for (int kr = 0; kr < 4; ++kr) a[n][kr] = __builtin_amdgcn_raw_buffer_load_b128(rs, WS_PO + gb_ * 2560 + m0[n] * 4, kr * 10240, 16);
#pragma unroll
        for (int n = 0; n < 2; ++n)
#pragma unroll
          for (int kr = 0; kr < 4; ++kr) bad |= bad16(a[n][kr]);
        if (wnone(bad) || ++spins > SPIN_CAP) break;
        __builtin_amdgcn_s_sleep(POLL_SLEEP);
      }
      STAMP(6);
#pragma unroll
      for (int n = 0; n < 2; ++n) {
        const f32x4 ao = (__builtin_bit_cast(f32x4, a[n][0]) + __builtin_bit_cast(f32x4, a[n][1])) + (__builtin_bit_cast(f32x4, a[n][2]) + __builtin_bit_cast(f32x4, a[n][3]));
        const float xv[4] = {bflo(x4[n].x), bfhi(x4[n].x), bflo(x4[n].y), bfhi(x4[n].y)};
        float y[4];
        const float live = (n == 0 || lane < 16) ? 1.f : 0.f;
#pragma unroll
        for (int e = 0; e < 4; ++e) {
          const float x2 = xv[e] + ga4[n][e] * ao[e];
          s1 += live * x2; s2 += live * x2 * x2;
          y[e] = x2 * cb4[n][e];
        }
        const unsigned h01 = pk2(y[0], y[1]), h23 = pk2(y[2], y[3]);
        const unsigned l01 = pk2(y[0] - bflo(h01), y[1] - bfhi(h01)), l23 = pk2(y[2] - bflo(h23), y[3] - bfhi(h23));
        lds_st<v2u>(L_GA + (4 * gb_ + 0) * 1296 + m0[n] * 2, v2u{h01, h23});
        lds_st<v2u>(L_GA + (4 * gb_ + 1) * 1296 + m0[n] * 2, v2u{l01, l23});
      }
      s1 = x32_sum(x16_sum(sum16(s1)));
      s2 = x32_sum(x16_sum(sum16(s2)));
      if (lane == 0) lds_st<v2u>(L_LN + w * 8, v2u{__float_as_uint(s1), __float_as_uint(s2)});
    }
    BAR();
    {
      f32x4 ac[4] = {zero4, zero4, zero4, zero4};
      v4u aa[3];
#pragma unroll
      for (int n = 0; n < 3; ++n) aa[n] = lds_ld<v4u>(L_GA + l15 * 1296 + (32 * min(w + 8 * n, 19) + 8 * g) * 2);
      __builtin_amdgcn_sched_barrier(0);
#pragma unroll
      for (int n = 0; n < 3; ++n) {
        if (w + 8 * n < 20) {
#pragma unroll
          for (int j = 0; j < 4; ++j) ac[j] = mfma32(aa[n], fg[n][j], ac[j]);
        }
      }
#pragma unroll
      for (int j = 0; j < 4; ++j) lds_st<float>(L_RG + (((w * 2 + 0) * 4 + g) * 64 + 16 * j + l15) * 4, ac[j][0] + ac[j][1]);
    }
    BAR();
    if (w < 4) {
      float v[8];
#pragma unroll
      for (int ww = 0; ww < 8; ++ww) v[ww] = lds_ld<float>(L_RG + (((ww * 2 + 0) * 4 + w) * 64 + lane) * 4);
      const v4u ln = lds_ld<v4u>(L_LN + w * 16);
      const float T = ((v[0] + v[1]) + (v[2] + v[3])) + ((v[4] + v[5]) + (v[6] + v[7]));
      const float s1 = __uint_as_float(ln.x) + __uint_as_float(ln.z), s2 = __uint_as_float(ln.y) + __uint_as_float(ln.w);
      const float mean = s1 * (1.f / DM);
      const float rstd = rsqrtf(fmaxf(s2 * (1.f / DM) - mean * mean, 0.f) + 1e-5f);
      const float val = rstd * (T - mean * U1) + U2;
      auto sw = __builtin_amdgcn_permlane32_swap(__float_as_uint(val), __float_as_uint(val), false, false);
      const float gv = __uint_as_float(sw[0]), uv = __uint_as_float(sw[1]);
      const float z = 0.7978845608028654f * (gv + 0.044715f * gv * gv * gv);
      const float th = 1.f - 2.f * __builtin_amdgcn_rcpf(1.f + __expf(2.f * z));
      const float hh = 0.5f * gv * (1.f + th) * uv;
      const unsigned hi = pk2(hh, 0.f) & 0xFFFFu;
      const unsigned lo = pk2(hh - bf2f(hi), 0.f) & 0xFFFFu;
      lds_st<u16>(L_HH + (4 * w + (lane >> 5)) * 80 + (lane & 31) * 2, (u16)(lane < 32 ? hi : lo));
    }
    BAR();
    {
      const v4u a = lds_ld<v4u>(L_HH + l15 * 80 + g * 16);
#pragma unroll
      for (int n = 0; n < 2; ++n) {
        const int u = w + 8 * n;
        if (u < 10) {
          f32x4 o;
#pragma unroll
          for (int j = 0; j < 4; ++j) { const f32x4 d = mfma32(a, fd[n][j], zero4); o[j] = d[0] + d[1]; }
          const v4u ov = {nosent(__float_as_uint(o[0])), nosent(__float_as_uint(o[1])), nosent(__float_as_uint(o[2])), nosent(__float_as_uint(o[3]))};
          st16s(rs, WS_PD + (8 * u + (l15 >> 1)) * 6912 + gp * 128 + g * 32 + (l15 & 1) * 16, ov);
        }
      }
    }
    STAMP(7);
    FLUSH_STAMPS();
  }
}

extern "C" void kernel_launch(void* const* d_in, const int* in_sizes, int n_in, void* d_out, int out_size, void* d_ws, size_t ws_size, hipStream_t stream) {
  Params p;
  p.x = (const u16*)d_in[0]; p.ckv = (const u16*)d_in[1]; p.wq = (const u16*)d_in[2]; p.wuk = (const u16*)d_in[3];
  p.wuv = (const u16*)d_in[4]; p.wo = (const u16*)d_in[5]; p.wg = (const u16*)d_in[7]; p.wu = (const u16*)d_in[8];
  p.wd = (const u16*)d_in[9]; p.c = (const u16*)d_in[10]; p.wmod = (const u16*)d_in[11];
  p.out = (u16*)d_out; p.ws = (unsigned char*)d_ws;
  (void)hipFuncSetAttribute((const void*)mega, hipFuncAttributeMaxDynamicSharedMemorySize, LDS_BYTES);
  hipLaunchKernelGGL(mega, dim3(256), dim3(512), LDS_BYTES, stream, p);
}
```

```cpp
#include <hip/hip_runtime.h>
#include <stdint.h>

typedef unsigned short u16;
typedef unsigned v4u __attribute__((ext_vector_type(4)));
typedef unsigned v2u __attribute__((ext_vector_type(2)));
typedef float f32x4 __attribute__((ext_vector_type(4)));
typedef __bf16 bf16x8 __attribute__((ext_vector_type(8)));
typedef short s16x4 __attribute__((ext_vector_type(4)));
#define GAS __attribute__((address_space(1)))
#define LAS __attribute__((address_space(3)))
#define DEV __device__ __forceinline__

#define ROT 7
#define H1_T 420
#define O_DELAY 30
#define ST_DELAY 60
#define KV_INFL 16
#define KV_DELAY 100
#define POLL_SLEEP 1

constexpr int NB = 4, NH = 32, SK = 4096, HD = 128, DM = 640, DC = 512, DF = 1728;
constexpr unsigned SENT = 0xFFFFFFFFu;
constexpr unsigned SPIN_CAP = 1u << 16;

constexpr int WS_EPOCH = 0;
constexpr int WS_CF1 = 0x180000 + 0x40000;
constexpr int WS_HB = 0x100000;
constexpr int WS_QP = 0x10000;
constexpr int WS_QB = 0x30000;
constexpr int WS_ST2 = 0x1A8000;
constexpr int WS_PX = 0x60000;
constexpr int WS_PO = 0x80000;
constexpr int WS_MP = 0x90000;
constexpr int WS_PD = 0x120000;
constexpr int WS_CK = 0x1E0000;
constexpr int WS_OB = 0x200000;
constexpr int WS_BYTES = 0x1000000;
constexpr int LDS_BYTES = 163840;

struct Params {
  const u16 *x, *ckv, *wq, *wuk, *wuv, *wo, *wg, *wu, *wd, *c, *wmod;
  u16* out;
  unsigned char* ws;
};

DEV float bf2f(unsigned b) { return __uint_as_float(b << 16); }
DEV float bflo(unsigned w) { return __uint_as_float(w << 16); }
DEV float bfhi(unsigned w) { return __uint_as_float(w & 0xFFFF0000u); }
DEV unsigned pk2(float lo, float hi) {
  typedef __bf16 bf2 __attribute__((ext_vector_type(2)));
  typedef float f2 __attribute__((ext_vector_type(2)));
  f2 v = {lo, hi};
  bf2 r = __builtin_convertvector(v, bf2);
  return __builtin_bit_cast(unsigned, r);
}
DEV unsigned nosent(unsigned v) { return v < 0xFFFFFFFEu ? v : 0xFFFFFFFEu; }
DEV f32x4 mfma32(v4u a, v4u b, f32x4 c) {
  return __builtin_amdgcn_mfma_f32_16x16x32_bf16(__builtin_bit_cast(bf16x8, a), __builtin_bit_cast(bf16x8, b), c, 0, 0, 0);
}
DEV f32x4 mfma16(v2u a, v2u b, f32x4 c) {
  return __builtin_amdgcn_mfma_f32_16x16x16bf16_1k(__builtin_bit_cast(s16x4, a), __builtin_bit_cast(s16x4, b), c, 0, 0, 0);
}
constexpr unsigned PLO = 0x05040100u, PHI = 0x07060302u;
DEV unsigned plo(unsigned hi_row, unsigned lo_row) { return __builtin_amdgcn_perm(hi_row, lo_row, PLO); }
DEV unsigned phi(unsigned hi_row, unsigned lo_row) { return __builtin_amdgcn_perm(hi_row, lo_row, PHI); }
DEV void tr8x8(const v4u (&v)[8], v4u (&f)[8]) {
#pragma unroll
  for (int q = 0; q < 4; ++q) {
#pragma unroll
    for (int t = 0; t < 4; ++t) {
      f[2 * q][t] = plo(v[2 * t + 1][q], v[2 * t][q]);
      f[2 * q + 1][t] = phi(v[2 * t + 1][q], v[2 * t][q]);
    }
  }
}
DEV void tr8x4(const v2u (&v)[8], v4u (&f)[4]) {
#pragma unroll
  for (int q = 0; q < 2; ++q) {
#pragma unroll
    for (int t = 0; t < 4; ++t) {
      f[2 * q][t] = plo(v[2 * t + 1][q], v[2 * t][q]);
      f[2 * q + 1][t] = phi(v[2 * t + 1][q], v[2 * t][q]);
    }
  }
}
DEV void tr8x2(const unsigned (&v)[8], v4u (&f)[2]) {
#pragma unroll
  for (int t = 0; t < 4; ++t) {
    f[0][t] = plo(v[2 * t + 1], v[2 * t]);
    f[1][t] = phi(v[2 * t + 1], v[2 * t]);
  }
}
DEV unsigned tagf(unsigned e) {
  e += 0x9E3779B9u; e ^= e >> 16; e *= 0x85EBCA6Bu; e ^= e >> 13; e *= 0xC2B2AE35u; e ^= e >> 16; return e;
}
#define BAR() asm volatile("s_waitcnt lgkmcnt(0)\n\ts_barrier" ::: "memory")
#define VMWAIT(n) asm volatile("s_waitcnt vmcnt(" #n ")" ::: "memory")
#define FENCE() asm volatile("" ::: "memory")
typedef __amdgpu_buffer_rsrc_t rsrc_t;
DEV v4u ld16s(rsrc_t rs, int off) { return __builtin_amdgcn_raw_buffer_load_b128(rs, off, 0, 16); }
DEV v2u ld8s(rsrc_t rs, int off) { return __builtin_amdgcn_raw_buffer_load_b64(rs, off, 0, 16); }
DEV unsigned ld4s(rsrc_t rs, int off) { return __builtin_amdgcn_raw_buffer_load_b32(rs, off, 0, 16); }
DEV void st16s(rsrc_t rs, int off, v4u v) { __builtin_amdgcn_raw_buffer_store_b128(v, rs, off, 0, 16); }
DEV void st8s(rsrc_t rs, int off, v2u v) { __builtin_amdgcn_raw_buffer_store_b64(v, rs, off, 0, 16); }
DEV void st4s(rsrc_t rs, int off, unsigned v) { __builtin_amdgcn_raw_buffer_store_b32(v, rs, off, 0, 16); }
DEV bool ok16(v4u v) {
  unsigned m = max(max(v.x, v.y), max(v.z, v.w));
  return m != SENT;
}
DEV bool ok8(v2u v) { return max(v.x, v.y) != SENT; }
template <typename T> DEV T ldg(const void* p) { return *(const GAS T*)p; }
template <typename T> DEV T ldnt(const void* p) { return __builtin_nontemporal_load((const GAS T*)p); }
template <typename T> DEV T lds_ld(unsigned off) { return *(const LAS T*)(uintptr_t)off; }
template <typename T> DEV void lds_st(unsigned off, T v) { *(LAS T*)(uintptr_t)off = v; }

DEV float dpp_xor1(float v) { return __builtin_bit_cast(float, __builtin_amdgcn_mov_dpp(__builtin_bit_cast(int, v), 0xB1, 0xf, 0xf, true)); }
DEV float dpp_xor2(float v) { return __builtin_bit_cast(float, __builtin_amdgcn_mov_dpp(__builtin_bit_cast(int, v), 0x4E, 0xf, 0xf, true)); }
DEV float dpp_ror4(float v) { return __builtin_bit_cast(float, __builtin_amdgcn_mov_dpp(__builtin_bit_cast(int, v), 0x124, 0xf, 0xf, true)); }
DEV float dpp_ror8(float v) { return __builtin_bit_cast(float, __builtin_amdgcn_mov_dpp(__builtin_bit_cast(int, v), 0x128, 0xf, 0xf, true)); }
DEV float sum16(float v) { v += dpp_xor1(v); v += dpp_xor2(v); v += dpp_ror4(v); v += dpp_ror8(v); return v; }
DEV float x16_sum(float x) {
  auto s = __builtin_amdgcn_permlane16_swap(__float_as_uint(x), __float_as_uint(x), false, false);
  return __uint_as_float(s[0]) + __uint_as_float(s[1]);
}
DEV float x32_sum(float x) {
  auto t = __builtin_amdgcn_permlane32_swap(__float_as_uint(x), __float_as_uint(x), false, false);
  return __uint_as_float(t[0]) + __uint_as_float(t[1]);
}
DEV float fmx(float a, float b) { return __builtin_amdgcn_fmed3f(a, b, __builtin_inff()); }
DEV float fmx3(float a, float b, float c) { return fmx(fmx(a, b), c); }
DEV float x16_max(float x) {
  auto s = __builtin_amdgcn_permlane16_swap(__float_as_uint(x), __float_as_uint(x), false, false);
  return fmx(__uint_as_float(s[0]), __uint_as_float(s[1]));
}
DEV float x32_max(float x) {
  auto t = __builtin_amdgcn_permlane32_swap(__float_as_uint(x), __float_as_uint(x), false, false);
  return fmx(__uint_as_float(t[0]), __uint_as_float(t[1]));
}
DEV float max16(float v) { v = fmaxf(v, dpp_xor1(v)); v = fmaxf(v, dpp_xor2(v)); v = fmaxf(v, dpp_ror4(v)); v = fmaxf(v, dpp_ror8(v)); return v; }
DEV f32x4 sel4(bool c, f32x4 a, f32x4 b) { f32x4 r; r[0] = c ? a[0] : b[0]; r[1] = c ? a[1] : b[1]; r[2] = c ? a[2] : b[2]; r[3] = c ? a[3] : b[3]; return r; }
DEV v2u sel2(bool c, v2u a, v2u b) { v2u r; r[0] = c ? a[0] : b[0]; r[1] = c ? a[1] : b[1]; return r; }
DEV bool wall(bool p) { return __builtin_amdgcn_ballot_w64(!p) == 0ull; }

DEV rsrc_t mkrs(const void* ptr, int bytes) { return __builtin_amdgcn_make_buffer_rsrc((void*)ptr, 0, bytes, 0x00020000); }
DEV v4u bl16(rsrc_t r, int voff, int soff) { return __builtin_amdgcn_raw_buffer_load_b128(r, voff, soff, 0); }
DEV v4u bl16nt(rsrc_t r, int voff, int soff) { return __builtin_amdgcn_raw_buffer_load_b128(r, voff, soff, 2); }
DEV v2u bl8(rsrc_t r, int voff, int soff) { return __builtin_amdgcn_raw_buffer_load_b64(r, voff, soff, 0); }
DEV unsigned bl4(rsrc_t r, int voff, int soff) { return __builtin_amdgcn_raw_buffer_load_b32(r, voff, soff, 0); }
DEV unsigned bad16(v4u v) { return (unsigned)(max(max(v.x, v.y), max(v.z, v.w)) == SENT); }
DEV bool wnone(unsigned bad) { return __builtin_amdgcn_ballot_w64(bad != 0u) == 0ull; }
DEV float silu(float v) { return v * __builtin_amdgcn_rcpf(1.f + __expf(-v)); }
DEV v4u silu_frag(v4u cv, bool lo_lane) {
  v4u o;
#pragma unroll
  for (int q = 0; q < 4; ++q) {
    const float a = silu(bflo(cv[q])), b = silu(bfhi(cv[q]));
    const unsigned hi = pk2(a, b);
    const unsigned lo = pk2(a - bflo(hi), b - bfhi(hi));
    o[q] = lo_lane ? lo : hi;
  }
  return o;
}

template <int NL> DEV void wait_flags(rsrc_t rs, int off, unsigned tag) {
  unsigned spins = 0;
  for (;;) {
    FENCE();
    unsigned bad = 0;
    unsigned v[NL];
#pragma unroll
    for (int i = 0; i < NL; ++i) v[i] = __builtin_amdgcn_raw_buffer_load_b32(rs, off, i * 8192, 16);
#pragma unroll
    for (int i = 0; i < NL; ++i) bad |= v[i] ^ tag;
    if (wnone(bad) || ++spins > SPIN_CAP) break;
    __builtin_amdgcn_s_sleep(6);
  }
}

template <bool NT> DEV void dma16(const void* gsrc, unsigned lds_base) {
  unsigned keep;
  if (NT) asm volatile("s_mov_b32 %0, m0\n\ts_mov_b32 m0, %2\n\ts_nop 0\n\tglobal_load_lds_dwordx4 %1, off nt\n\ts_mov_b32 m0, %0" : "=&s"(keep) : "v"(gsrc), "s"(lds_base) : "memory");
  else asm volatile("s_mov_b32 %0, m0\n\ts_mov_b32 m0, %2\n\ts_nop 0\n\tglobal_load_lds_dwordx4 %1, off\n\ts_mov_b32 m0, %0" : "=&s"(keep) : "v"(gsrc), "s"(lds_base) : "memory");
}
DEV void dma_kstep64(const u16* src_row0, int row_stride_elems, int lane, unsigned lds_base) {
  const u16* s = src_row0 + (size_t)(lane >> 2) * row_stride_elems + (lane & 3) * 8;
  dma16<false>(s, lds_base);
  dma16<false>(s + (size_t)16 * row_stride_elems, lds_base + 1024);
}
DEV void tr_kstep64(unsigned lds_base, int lane, v2u (&t)[4]) {
  const unsigned a = lds_base + (8 * (lane >> 4) + ((lane & 15) >> 2)) * 64 + (lane & 3) * 8;
  asm volatile("ds_read_b64_tr_b16 %0, %1" : "=v"(t[0]) : "v"(a) : "memory");
  asm volatile("ds_read_b64_tr_b16 %0, %1 offset:256" : "=v"(t[1]) : "v"(a) : "memory");
  asm volatile("ds_read_b64_tr_b16 %0, %1 offset:32" : "=v"(t[2]) : "v"(a) : "memory");
  asm volatile("ds_read_b64_tr_b16 %0, %1 offset:288" : "=v"(t[3]) : "v"(a) : "memory");
}
DEV void dma_kstep128(const u16* src_row0, int row_stride_elems, int lane, unsigned lds_base) {
  const u16* s = src_row0 + (size_t)(lane >> 3) * row_stride_elems + (lane & 7) * 8;
#pragma unroll
  for (int i = 0; i < 4; ++i) dma16<false>(s + (size_t)(8 * i) * row_stride_elems, lds_base + i * 1024);
}
DEV void tr_kstep128(unsigned lds_base, int lane, v2u (&t)[8]) {
  const unsigned a = lds_base + (8 * (lane >> 4) + ((lane & 15) >> 2)) * 128 + (lane & 3) * 8;
  asm volatile("ds_read_b64_tr_b16 %0, %1" : "=v"(t[0]) : "v"(a) : "memory");
  asm volatile("ds_read_b64_tr_b16 %0, %1 offset:512" : "=v"(t[1]) : "v"(a) : "memory");
  asm volatile("ds_read_b64_tr_b16 %0, %1 offset:32" : "=v"(t[2]) : "v"(a) : "memory");
  asm volatile("ds_read_b64_tr_b16 %0, %1 offset:544" : "=v"(t[3]) : "v"(a) : "memory");
  asm volatile("ds_read_b64_tr_b16 %0, %1 offset:64" : "=v"(t[4]) : "v"(a) : "memory");
  asm volatile("ds_read_b64_tr_b16 %0, %1 offset:576" : "=v"(t[5]) : "v"(a) : "memory");
  asm volatile("ds_read_b64_tr_b16 %0, %1 offset:96" : "=v"(t[6]) : "v"(a) : "memory");
  asm volatile("ds_read_b64_tr_b16 %0, %1 offset:608" : "=v"(t[7]) : "v"(a) : "memory");
}
#define TRWAIT8(t) asm volatile("s_waitcnt lgkmcnt(0)" : "+v"(t[0]), "+v"(t[1]), "+v"(t[2]), "+v"(t[3]), "+v"(t[4]), "+v"(t[5]), "+v"(t[6]), "+v"(t[7]) :: "memory")
DEV v4u frag8_of(const v2u (&t)[8], int nt) { return v4u{t[2 * nt].x, t[2 * nt].y, t[2 * nt + 1].x, t[2 * nt + 1].y}; }
#define TRWAIT4(t) asm volatile("s_waitcnt lgkmcnt(0)" : "+v"(t[0]), "+v"(t[1]), "+v"(t[2]), "+v"(t[3]) :: "memory")
DEV v4u frag_of(const v2u (&t)[4], int nt) { return v4u{t[2 * nt].x, t[2 * nt].y, t[2 * nt + 1].x, t[2 * nt + 1].y}; }

DEV void f_dma(const Params& p, int ft, int w, int lane, unsigned l_wf) {
  const int kr = ft / 10, ms = ft % 10;
  const u16* src = p.wo + (size_t)(512 * kr + 64 * w) * DM + 64 * ms;
  dma_kstep128(src, DM, lane, l_wf + w * 8192);
  dma_kstep128(src + (size_t)32 * DM, DM, lane, l_wf + w * 8192 + 4096);
}
DEV void f_frags(int w, int lane, unsigned l_wf, v4u (&ff)[2][4]) {
  VMWAIT(0);
#pragma unroll
  for (int ks = 0; ks < 2; ++ks) {
    v2u t[8];
    tr_kstep128(l_wf + w * 8192 + ks * 4096, lane, t);
    TRWAIT8(t);
#pragma unroll
    for (int j = 0; j < 4; ++j) ff[ks][j] = frag8_of(t, j);
  }
}
DEV int f_poff(int ft, int w, int l15, int g) {
  return WS_PX + (((4 * (ft / 10) + (w >> 1)) * 4 + (l15 & 3)) * 4 + (l15 >> 2)) * 256 + (w & 1) * 128 + g * 16;
}
DEV void f_run(rsrc_t rs, int ft, int tid, int w, int l15, int g, const v4u (&ff)[2][4], unsigned l_rf) {
  const f32x4 zero4 = {0.f, 0.f, 0.f, 0.f};
  const int kr = ft / 10, ms = ft % 10;
  v4u pa0, pa1;
  unsigned spins = 0;
  const int poff = f_poff(ft, w, l15, g);
  for (;;) {
    FENCE();
    pa0 = ld16s(rs, poff); pa1 = ld16s(rs, poff + 64);
    if (wnone(bad16(pa0) | bad16(pa1)) || ++spins > SPIN_CAP) break;
    __builtin_amdgcn_s_sleep(POLL_SLEEP);
  }
  f32x4 d[4];
#pragma unroll
  for (int j = 0; j < 4; ++j) d[j] = mfma32(pa0, ff[0][j], zero4);
#pragma unroll
  for (int j = 0; j < 4; ++j) d[j] = mfma32(pa1, ff[1][j], d[j]);
#pragma unroll
  for (int j = 0; j < 4; ++j) lds_st<float>(l_rf + ((w * 4 + g) * 64 + 16 * j + l15) * 4, (d[j][0] + d[j][1]) + (d[j][2] + d[j][3]));
  BAR();
  if (w < 2) {
    const int b = tid >> 5, pr = tid & 31;
    v2u v[8];
#pragma unroll
    for (int ww = 0; ww < 8; ++ww) v[ww] = lds_ld<v2u>(l_rf + ((ww * 4 + b) * 64 + 2 * pr) * 4);
    const float s0 = ((__uint_as_float(v[0].x) + __uint_as_float(v[1].x)) + (__uint_as_float(v[2].x) + __uint_as_float(v[3].x))) + ((__uint_as_float(v[4].x) + __uint_as_float(v[5].x)) + (__uint_as_float(v[6].x) + __uint_as_float(v[7].x)));
    const float s1 = ((__uint_as_float(v[0].y) + __uint_as_float(v[1].y)) + (__uint_as_float(v[2].y) + __uint_as_float(v[3].y))) + ((__uint_as_float(v[4].y) + __uint_as_float(v[5].y)) + (__uint_as_float(v[6].y) + __uint_as_float(v[7].y)));
    st4s(rs, WS_PO + (kr * 4 + b) * 1280 + (64 * ms + 2 * pr) * 2, nosent(pk2(s0, s1)));
  }
}
DEV void f_clear(rsrc_t rs, int ft, int lane) {
  const v4u sent4 = {SENT, SENT, SENT, SENT};
  if (lane < 32) st16s(rs, WS_PO + ((ft / 10) * 4 + (lane >> 3)) * 1280 + (ft % 10) * 128 + (lane & 7) * 16, sent4);
}

__global__ void __launch_bounds__(512, 2) mega(Params p) {
  extern __shared__ __attribute__((aligned(16))) unsigned char smem_[];
  const unsigned long long t_start = __builtin_amdgcn_s_memrealtime();
  const int tid = threadIdx.x, lane = tid & 63;
  const int w = __builtin_amdgcn_readfirstlane(tid >> 6);
  const int x8 = blockIdx.x & 7, slot = blockIdx.x >> 3;
  const int xg = (x8 + ROT) & 7;
  const rsrc_t rs = mkrs(p.ws, WS_BYTES);
  const int l15 = lane & 15, g = lane >> 4;
  unsigned epoch, tag;
#define TAGINIT() do { epoch = *(const __attribute__((address_space(4))) unsigned*)(p.ws + WS_EPOCH); asm volatile("" : "+s"(epoch)); tag = tagf(epoch); } while (0)
  const v4u sent4 = {SENT, SENT, SENT, SENT};
  const f32x4 zero4 = {0.f, 0.f, 0.f, 0.f};

  if (xg < 4) {
    const int h = xg * 8 + (slot >> 2), q4 = slot & 3;
    const bool isA = slot < 20;
    const int r = xg * 20 + slot;
    const int mi = xg * 12 + (slot - 20);
    const bool isM = !isA && mi < 40, isF = isA;
    const int ft = r;
    constexpr unsigned L_RA = 0;
    constexpr unsigned L_HA = 8192;
    constexpr unsigned L_RB = 16384;
    constexpr unsigned L_QS = 27648;
    constexpr unsigned L_W = 28672;
    constexpr unsigned L_CC = 30720;
    constexpr unsigned L_RF = 49152;
    constexpr unsigned L_R2 = 40960;
    constexpr unsigned L_QA = 45056;
    constexpr unsigned L_WF = 65536;
    const rsrc_t rs_x = mkrs(p.x, NB * DM * 2);
    const rsrc_t rs_c = mkrs(p.c, NB * DM * 2);

    v4u fq[8];
    v4u vk[4];
    const rsrc_t rs_wuk = mkrs(p.wuk, DC * NH * HD * 2);
    const int koff = (((128 * q4 + 16 * w + l15) * NH + h) * HD + 8 * g) * 2;
    if (w < 5) {
      const rsrc_t rs_wq = mkrs(p.wq, DM * NH * HD * 2);
      const int qoff = ((160 * q4 + 32 * w + 8 * g) * (NH * HD) + HD * h + 8 * l15) * 2;
      v4u vq[8];
      if (isA) {
        const rsrc_t rs_wmod = mkrs(p.wmod, DM * 6 * DM * 2);
        const v4u cf = bl16(rs_c, ((l15 >> 2) * DM + 160 * xg + 32 * w + 8 * g) * 2, 0);
        v2u va[8];
        const int aoff = ((160 * xg + 32 * w + 8 * g) * (6 * DM) + 64 * slot + 4 * l15) * 2;
#pragma unroll
        for (int i = 0; i < 8; ++i) va[i] = __builtin_amdgcn_raw_buffer_load_b64(rs_wmod, aoff, i * (6 * DM * 2), 2);
        __builtin_amdgcn_sched_barrier(0);
        __builtin_amdgcn_s_barrier();
        __builtin_amdgcn_sched_barrier(0);
#pragma unroll
        for (int i = 0; i < 8; ++i) vq[i] = bl16nt(rs_wq, qoff, i * (NH * HD * 2));
        __builtin_amdgcn_sched_barrier(0);
        TAGINIT();
        const v4u af = silu_frag(cf, lane & 1);
        v4u fa[4];
        tr8x4(va, fa);
        f32x4 o;
#pragma unroll
        for (int j = 0; j < 4; ++j) { const f32x4 d = mfma32(af, fa[j], zero4); o[j] = d[0] + d[1]; }
        lds_st<f32x4>(L_RA + ((w * 4 + g) * 64 + 4 * l15) * 4, o);
        BAR();
        if (w < 2) {
          const int b = tid >> 5, pr = tid & 31;
          v2u t2[5];
#pragma unroll
          for (int ww = 0; ww < 5; ++ww) t2[ww] = lds_ld<v2u>(L_RA + ((ww * 4 + b) * 64 + 2 * pr) * 4);
          const float s0 = ((__uint_as_float(t2[0].x) + __uint_as_float(t2[1].x)) + (__uint_as_float(t2[2].x) + __uint_as_float(t2[3].x))) + __uint_as_float(t2[4].x);
          const float s1 = ((__uint_as_float(t2[0].y) + __uint_as_float(t2[1].y)) + (__uint_as_float(t2[2].y) + __uint_as_float(t2[3].y))) + __uint_as_float(t2[4].y);
          const int part = slot >= 10, cbk = slot - 10 * part;
          st8s(rs, WS_HB + (((part * 4 + xg) * 4 + b) * 320 + 32 * cbk + pr) * 8, v2u{pk2(s0, s1), tag});
        }
      } else {
        __builtin_amdgcn_s_barrier();
        __builtin_amdgcn_sched_barrier(0);
#pragma unroll
        for (int i = 0; i < 8; ++i) vq[i] = bl16nt(rs_wq, qoff, i * (NH * HD * 2));
        __builtin_amdgcn_sched_barrier(0);
        TAGINIT();
      }
      tr8x8(vq, fq);
      if (w == 3) {
        wait_flags<1>(rs, WS_CF1 + (((4 * (slot >> 2) + (lane & 3)) << 3) + x8) * 128, tag);
        const int qpoff = WS_QP + h * 4096 + lane * 16;
        v4u t0 = ld16s(rs, qpoff), t1 = ld16s(rs, qpoff + 1024), t2 = ld16s(rs, qpoff + 2048), t3 = ld16s(rs, qpoff + 3072);
        asm volatile("" :: "v"(t0), "v"(t1), "v"(t2), "v"(t3));
      }
    } else {
      const int v3 = w - 5;
      const int mq = min(16 * v3 + l15, 39);
      if (w == 7) {
        st16s(rs, WS_QP + (h * 4 + q4) * 1024 + lane * 16, sent4);
        st16s(rs, WS_PX + (h * 4 + q4) * 1024 + lane * 16, sent4);
        st16s(rs, WS_QB + (g * 32 + h) * 1024 + q4 * 256 + l15 * 16, sent4);
        if (isM) st16s(rs, WS_MP + ((mi / 10) * 4 + g) * 2560 + (mi % 10) * 256 + l15 * 16, sent4);
        if (isF) f_clear(rs, ft, lane);
      }
      v4u xs[5];
#pragma unroll
      for (int it = 0; it < 5; ++it) xs[it] = bl16(rs_x, (g * DM + 8 * l15) * 2, it * 256);
      const v2u xq = bl8(rs_x, (g * DM + 160 * q4 + 4 * mq) * 2, 0);
      __builtin_amdgcn_sched_barrier(0);
      __builtin_amdgcn_s_barrier();
      __builtin_amdgcn_sched_barrier(0);
      TAGINIT();
      if (w == 7) {
        VMWAIT(0);
        if (lane == 0) st4s(rs, WS_CF1 + blockIdx.x * 128, tag);
      }
      if (isA) BAR();
      float s1 = 0.f, s2 = 0.f;
#pragma unroll
      for (int it = 0; it < 5; ++it)
#pragma unroll
        for (int q = 0; q < 4; ++q) { const float a = bflo(xs[it][q]), b2 = bfhi(xs[it][q]); s1 += a + b2; s2 += a * a + b2 * b2; }
      s1 = sum16(s1); s2 = sum16(s2);
      const float mean = s1 * (1.f / DM);
      const float rstd = rsqrtf(fmaxf(s2 * (1.f / DM) - mean * mean, 0.f) + 1e-5f);
      float xn[4] = {(bflo(xq.x) - mean) * rstd, (bfhi(xq.x) - mean) * rstd, (bflo(xq.y) - mean) * rstd, (bfhi(xq.y) - mean) * rstd};
      while (__builtin_amdgcn_s_memrealtime() - t_start < H1_T) __builtin_amdgcn_s_sleep(2);
      v4u hv[8];
      const int hoff = WS_HB + (g * 320 + 80 * q4 + 2 * mq) * 8;
      unsigned spins = 0;
      for (;;) {
        FENCE();
        unsigned bad = 0;
#pragma unroll
        for (int i = 0; i < 8; ++i) hv[i] = __builtin_amdgcn_raw_buffer_load_b128(rs, hoff, i * 10240, 16);
#pragma unroll
        for (int i = 0; i < 8; ++i) bad |= (hv[i].y ^ tag) | (hv[i].w ^ tag);
        if (wnone(bad) || ++spins > SPIN_CAP) break;
        __builtin_amdgcn_s_sleep(POLL_SLEEP);
      }
      float hh[4];
#pragma unroll
      for (int e = 0; e < 4; ++e) {
        const int d = (e >> 1) * 2;
        float sa, ca;
        if (e & 1) {
          sa = (bfhi(hv[0][d]) + bfhi(hv[1][d])) + (bfhi(hv[2][d]) + bfhi(hv[3][d]));
          ca = (bfhi(hv[4][d]) + bfhi(hv[5][d])) + (bfhi(hv[6][d]) + bfhi(hv[7][d]));
        } else {
          sa = (bflo(hv[0][d]) + bflo(hv[1][d])) + (bflo(hv[2][d]) + bflo(hv[3][d]));
          ca = (bflo(hv[4][d]) + bflo(hv[5][d])) + (bflo(hv[6][d]) + bflo(hv[7][d]));
        }
        hh[e] = xn[e] * ca + (xn[e] + sa);
      }
      const unsigned h01 = pk2(hh[0], hh[1]), h23 = pk2(hh[2], hh[3]);
      const unsigned l01 = pk2(hh[0] - bflo(h01), hh[1] - bfhi(h01)), l23 = pk2(hh[2] - bflo(h23), hh[3] - bfhi(h23));
      lds_st<v2u>(L_HA + (4 * g + 0) * 336 + mq * 8, v2u{h01, h23});
      lds_st<v2u>(L_HA + (4 * g + 1) * 336 + mq * 8, v2u{l01, l23});
    }
#pragma unroll
    for (int ks = 0; ks < 4; ++ks) vk[ks] = bl16nt(rs_wuk, koff, ks * 64);
    __builtin_amdgcn_sched_barrier(0);
    BAR();
    if (w == 7) {
      BAR();
      const int qpoff = WS_QP + ((h * 4 + (l15 & 3)) * 4 + (l15 >> 2)) * 256 + g * 16;
      v4u qa[4];
      unsigned spins = 0;
      for (;;) {
        FENCE();
#pragma unroll
        for (int ks = 0; ks < 4; ++ks) qa[ks] = __builtin_amdgcn_raw_buffer_load_b128(rs, qpoff + ks * 64, 0, 16);
        if (wnone(bad16(qa[0]) | bad16(qa[1]) | bad16(qa[2]) | bad16(qa[3])) || ++spins > SPIN_CAP) break;
      }
#pragma unroll
      for (int ks = 0; ks < 4; ++ks) lds_st<v4u>(L_QA + ks * 1024 + lane * 16, qa[ks]);
    } else {
      if (w < 5) {
        const v4u a = lds_ld<v4u>(L_HA + l15 * 336 + (32 * w + 8 * g) * 2);
        float o[8];
#pragma unroll
        for (int j = 0; j < 8; ++j) { const f32x4 d = mfma32(a, fq[j], zero4); o[j] = d[0] + d[1]; }
        const unsigned off = L_RB + ((w * 4 + g) * 128 + 8 * l15) * 4;
        lds_st<f32x4>(off, f32x4{o[0], o[1], o[2], o[3]});
        lds_st<f32x4>(off + 16, f32x4{o[4], o[5], o[6], o[7]});
      }
      BAR();
      if (w < 4) {
        v2u t2[5];
#pragma unroll
        for (int ww = 0; ww < 5; ++ww) t2[ww] = lds_ld<v2u>(L_RB + ((ww * 4 + w) * 128 + 2 * lane) * 4);
        const float s0 = ((__uint_as_float(t2[0].x) + __uint_as_float(t2[1].x)) + (__uint_as_float(t2[2].x) + __uint_as_float(t2[3].x))) + __uint_as_float(t2[4].x);
        const float s1 = ((__uint_as_float(t2[0].y) + __uint_as_float(t2[1].y)) + (__uint_as_float(t2[2].y) + __uint_as_float(t2[3].y))) + __uint_as_float(t2[4].y);
        st4s(rs, WS_QP + ((h * 4 + q4) * 4 + w) * 256 + lane * 4, nosent(pk2(s0, s1)));
      }
    }
    BAR();
    {
      v4u qa[4];
#pragma unroll
      for (int ks = 0; ks < 4; ++ks) qa[ks] = lds_ld<v4u>(L_QA + ks * 1024 + lane * 16);
      __builtin_amdgcn_sched_barrier(0);
      f32x4 d0 = mfma32(qa[0], vk[0], zero4), d1 = mfma32(qa[1], vk[1], zero4);
      d0 = mfma32(qa[2], vk[2], d0); d1 = mfma32(qa[3], vk[3], d1);
      d0 += d1;
      const float qv = ((d0[0] + d0[1]) + (d0[2] + d0[3])) * (0.08838834764831845f * 1.4426950408889634f);
      lds_st<u16>(L_QS + (g * 128 + 16 * w + l15) * 2, (u16)(pk2(qv, 0.f) & 0xFFFFu));
      BAR();
      if (w == 0) {
        v4u v = lds_ld<v4u>(L_QS + lane * 16);
        v.x = nosent(v.x); v.y = nosent(v.y); v.z = nosent(v.z); v.w = nosent(v.w);
        st16s(rs, WS_QB + (g * 32 + h) * 1024 + q4 * 256 + l15 * 16, v);
      }
    }
    const int obase = WS_OB + (h * 4 + q4) * 32768;
    v4u fe[4][4];
    v4u ff[2][4];
    v2u ve[4][8];
    if (w < 2) {
      const rsrc_t rs_wuv = mkrs(p.wuv, DC * NH * HD * 2);
      const int off = (((128 * q4 + 4 * (g >> 1) + 16 * (g & 1)) * NH + h) * HD + 64 * w + 4 * l15) * 2;
#pragma unroll
      for (int ks = 0; ks < 4; ++ks)
#pragma unroll
        for (int i = 0; i < 8; ++i) ve[ks][i] = bl8(rs_wuv, off, (32 * ks + 8 * (i >> 2) + (i & 3)) * (NH * HD * 2));
    }
    v4u cf[3];
    if (isF) f_dma(p, ft, w, lane, L_WF);
    if (isM) {
      const int pidx = mi / 10;
      const int mcol = (pidx == 0 ? 2 : (pidx == 1 ? 5 : (pidx == 2 ? 3 : 4))) * DM + 64 * (mi % 10);
#pragma unroll
      for (int n = 0; n < 3; ++n) {
        const int kb = min(w + 8 * n, 19);
        cf[n] = bl16(rs_c, ((l15 >> 2) * DM + 32 * kb + 8 * g) * 2, 0);
        if (w + 8 * n < 20) dma_kstep128(p.wmod + (size_t)(32 * kb) * (6 * DM) + mcol, 6 * DM, lane, L_WF + kb * 4096);
      }
    }
    if (w < 2) {
#pragma unroll
      for (int ks = 0; ks < 4; ++ks) tr8x4(ve[ks], fe[ks]);
    }
    if (isF) f_frags(w, lane, L_WF, ff);
    if (isM) {
      const int pidx = mi / 10;
      f32x4 ac[4] = {zero4, zero4, zero4, zero4};
      VMWAIT(0);
#pragma unroll
      for (int n = 0; n < 3; ++n) {
        if (w + 8 * n < 20) {
          v2u t[8];
          tr_kstep128(L_WF + (w + 8 * n) * 4096, lane, t);
          const v4u af = silu_frag(cf[n], lane & 1);
          TRWAIT8(t);
#pragma unroll
          for (int j = 0; j < 4; ++j) ac[j] = mfma32(af, frag8_of(t, j), ac[j]);
        }
      }
#pragma unroll
      for (int j = 0; j < 4; ++j) lds_st<float>(L_RA + ((w * 4 + g) * 64 + 16 * j + l15) * 4, ac[j][0] + ac[j][1]);
      BAR();
      if (w < 4) {
        float sum = pidx == 3 ? 1.f : 0.f;
#pragma unroll
        for (int ww = 0; ww < 8; ++ww) sum += lds_ld<float>(L_RA + ((ww * 4 + w) * 64 + lane) * 4);
        st4s(rs, WS_MP + (pidx * 4 + w) * 2560 + (64 * (mi % 10) + lane) * 4, nosent(__float_as_uint(sum)));
      }
    }
    if (isF && w == 7) wait_flags<1>(rs, WS_CF1 + ((16 * ((ft / 10) & 1) + (lane & 15)) * 8 + x8) * 128, tag);
    BAR();
    if (isF) {
      v4u t0 = ld16s(rs, f_poff(ft, w, l15, g));
      asm volatile("" :: "v"(t0));
    }
    {
      const int b = w >> 1, sh = w & 1;
      unsigned spins = 0;
      const int ooff = obase + (b * 32 + 16 * sh + 4 * g) * 256 + l15 * 16;
      if (sh == 0) {
        v4u st;
        {
          const int coff = WS_ST2 + ((b * 32) * 32 + h) * 16;
          for (;;) {
            FENCE();
            st = ld16s(rs, coff);
            if (wnone((st.y ^ tag) | (st.w ^ tag)) || ++spins > SPIN_CAP) break;
            __builtin_amdgcn_s_sleep(3);
          }
        }
        { const unsigned long long t_c = __builtin_amdgcn_s_memrealtime(); while (__builtin_amdgcn_s_memrealtime() - t_c < ST_DELAY) __builtin_amdgcn_s_sleep(1); }
        const int stoff = WS_ST2 + ((b * 32 + (lane & 31)) * 32 + h) * 16;
        for (;;) {
          FENCE();
          st = ld16s(rs, stoff);
          if (wnone((st.y ^ tag) | (st.w ^ tag)) || ++spins > SPIN_CAP) break;
          __builtin_amdgcn_s_sleep(POLL_SLEEP + 1);
        }
        const float m = __uint_as_float(st.x), l = __uint_as_float(st.z);
        const float M = x16_max(max16(m));
        const float e = __builtin_amdgcn_exp2f(m - M);
        const float L = x16_sum(sum16(l * e));
        lds_st<float>(L_W + b * 128 + (lane & 31) * 4, e * __builtin_amdgcn_rcpf(L));
      }
      BAR();
      const unsigned long long t_st = __builtin_amdgcn_s_memrealtime();
      v4u o[4];
      const f32x4 w4 = lds_ld<f32x4>(L_W + b * 128 + (16 * sh + 4 * g) * 4);
      v2u aw;
      {
        const unsigned h01 = pk2(w4[0], w4[1]), h23 = pk2(w4[2], w4[3]);
        const unsigned l01 = pk2(w4[0] - bflo(h01), w4[1] - bfhi(h01)), l23 = pk2(w4[2] - bflo(h23), w4[3] - bfhi(h23));
        aw = sel2(l15 == 0, v2u{h01, h23}, sel2(l15 == 1, v2u{l01, l23}, v2u{0u, 0u}));
      }
      asm volatile("" : "+v"(aw));
      while (__builtin_amdgcn_s_memrealtime() - t_st < O_DELAY) __builtin_amdgcn_s_sleep(1);
      const int ckoff = WS_CK + (b * 32 + 16 * sh + 4 * g) * 1024 + (h * 8 + 2 * q4 + (l15 >> 3)) * 4;
      for (;;) {
        FENCE();
        unsigned ckv[4];
#pragma unroll
        for (int i = 0; i < 4; ++i) o[i] = __builtin_amdgcn_raw_buffer_load_b128(rs, ooff + i * 256, 0, 16);
#pragma unroll
        for (int i = 0; i < 4; ++i) ckv[i] = __builtin_amdgcn_raw_buffer_load_b32(rs, ckoff + i * 1024, 0, 16);
        unsigned bad = 0;
#pragma unroll
        for (int i = 0; i < 4; ++i) {
          unsigned f = (o[i][0] ^ o[i][1]) ^ (o[i][2] ^ o[i][3]);
          f ^= (unsigned)__builtin_amdgcn_mov_dpp((int)f, 0xB1, 0xf, 0xf, true);
          f ^= (unsigned)__builtin_amdgcn_mov_dpp((int)f, 0x4E, 0xf, 0xf, true);
          f ^= (unsigned)__builtin_amdgcn_mov_dpp((int)f, 0x141, 0xf, 0xf, true);
          bad |= f ^ tag ^ ckv[i];
        }
        if (wnone(bad) || ++spins > SPIN_CAP) break;
        __builtin_amdgcn_s_sleep(POLL_SLEEP + 2);
      }
      float cc[8];
#pragma unroll
      for (int q = 0; q < 4; ++q) {
        const v2u b0 = {plo(o[1][q], o[0][q]), plo(o[3][q], o[2][q])};
        const v2u b1 = {phi(o[1][q], o[0][q]), phi(o[3][q], o[2][q])};
        const f32x4 d0 = mfma16(aw, b0, zero4);
        const f32x4 d1 = mfma16(aw, b1, zero4);
        cc[2 * q] = d0[0] + d0[1];
        cc[2 * q + 1] = d1[0] + d1[1];
      }
      if (lane < 16) lds_st<v4u>(L_CC + (4 * b + sh) * 272 + lane * 16, v4u{pk2(cc[0], cc[1]), pk2(cc[2], cc[3]), pk2(cc[4], cc[5]), pk2(cc[6], cc[7])});
      BAR();
      if (w < 2) {
        f32x4 d[4] = {zero4, zero4, zero4, zero4};
        v4u a[4];
#pragma unroll
        for (int ks = 0; ks < 4; ++ks) a[ks] = lds_ld<v4u>(L_CC + l15 * 272 + (32 * ks + 8 * g) * 2);
        __builtin_amdgcn_sched_barrier(0);
#pragma unroll
        for (int ks = 0; ks < 4; ++ks)
#pragma unroll
          for (int j = 0; j < 4; ++j) d[j] = mfma32(a[ks], fe[ks][j], d[j]);
        st8s(rs, WS_PX + ((h * 4 + q4) * 4 + g) * 256 + (64 * w + 4 * l15) * 2,
             v2u{nosent(pk2(d[0][0] + d[0][1], d[1][0] + d[1][1])), nosent(pk2(d[2][0] + d[2][1], d[3][0] + d[3][1]))});
      }
    }
    if (!isA) {
      return;
    }
    f_run(rs, ft, tid, w, l15, g, ff, L_RF);
    if (w == 6) wait_flags<4>(rs, WS_CF1 + lane * 128, tag);
    BAR();

    float r2_x = 0.f, r2_gb = 0.f;
    if (w == 7) {
      const int b = (lane >> 3) & 3, j = lane & 7;
      unsigned spins = 0;
      unsigned a[8], ga, gb;
      const int o1 = (b * 640 + 8 * r + j) * 4;
      const int o2 = WS_PO + b * 1280 + (8 * r + (j & ~1)) * 2;
      const unsigned xr = __builtin_amdgcn_raw_buffer_load_b16(rs_x, (b * DM + 8 * r + j) * 2, 0, 0);
      for (;;) {
        FENCE();
        unsigned mx = 0;
#pragma unroll
        for (int k8 = 0; k8 < 8; ++k8) a[k8] = __builtin_amdgcn_raw_buffer_load_b32(rs, o2, k8 * 5120, 16);
        ga = ld4s(rs, WS_MP + 0 * 10240 + o1);
        gb = ld4s(rs, WS_MP + 1 * 10240 + o1);
#pragma unroll
        for (int k8 = 0; k8 < 8; ++k8) mx = max(mx, a[k8]);
        if (wnone((unsigned)(max(mx, max(ga, gb)) == SENT)) || ++spins > SPIN_CAP) break;
        __builtin_amdgcn_s_sleep(6);
      }
      r2_gb = __uint_as_float(gb);
      float ao = 0.f;
#pragma unroll
      for (int k8 = 0; k8 < 8; ++k8) ao += (j & 1) ? bfhi(a[k8]) : bflo(a[k8]);
      r2_x = bf2f(xr) + __uint_as_float(ga) * ao;
      BAR();
    } else {
      BAR();
      unsigned spins = 0;
      v4u t4;
      const int pdoff = WS_PD + r * 6912 + min(tid, 431) * 16;
      for (;;) {
        FENCE();
        t4 = ld16s(rs, pdoff);
        if (wnone(bad16(t4)) || ++spins > SPIN_CAP) break;
        __builtin_amdgcn_s_sleep(POLL_SLEEP);
      }
      f32x4 v = __builtin_bit_cast(f32x4, t4);
      if (tid >= 432) v = zero4;
#pragma unroll
      for (int e = 0; e < 4; ++e) { float s = v[e]; s += dpp_ror8(s); s = x16_sum(s); s = x32_sum(s); v[e] = s; }
      if (lane < 8) lds_st<f32x4>(L_R2 + (w * 8 + lane) * 16, v);
    }
    BAR();
    if (w == 7 && lane < 32) {
      const int b = lane >> 3, j = lane & 7;
      float v[7];
#pragma unroll
      for (int ww = 0; ww < 7; ++ww) v[ww] = lds_ld<float>(L_R2 + ((ww * 8 + b * 2 + (j >> 2)) * 4 + (j & 3)) * 4);
      const float s = ((v[0] + v[1]) + (v[2] + v[3])) + ((v[4] + v[5]) + v[6]);
      const float o = r2_x + r2_gb * s;
      *(GAS u16*)(p.out + b * DM + 8 * r + j) = (u16)(pk2(o, 0.f) & 0xFFFFu);
    }
    if (r == 0 && tid == 0) st4s(rs, WS_EPOCH, epoch + 1u);
  } else {
    const int b = xg - 4, s = slot;
    const int gp = b * 14 + slot;
    const bool isG = slot < 14 && gp < 54;

    constexpr unsigned L_KV = 0, L_Q = 131072;
    constexpr unsigned L_MX = 0;
    constexpr unsigned L_SM = 1024;
    constexpr unsigned L_P = 2048;
    constexpr unsigned L_WS = 16384;
    if (KV_DELAY) { while (__builtin_amdgcn_s_memrealtime() - t_start < KV_DELAY) __builtin_amdgcn_s_sleep(2); }
    TAGINIT();
    if (w == 7) {
      if (isG) {
        const int o0 = WS_PD + (lane >> 3) * 6912 + gp * 128 + (lane & 7) * 16;
#pragma unroll
        for (int i = 0; i < 10; ++i) __builtin_amdgcn_raw_buffer_store_b128(sent4, rs, o0, i * 8 * 6912, 16);
      }
    }
    {
      const u16* kvb = p.ckv + ((size_t)b * SK + s * 128 + 16 * w) * DC;
#pragma unroll
      for (int i = 0; i < 16; ++i) {
        dma16<true>(kvb + (size_t)i * DC + ((lane ^ i) * 8), L_KV + (16 * w + i) * 1024);
        if (i + 1 == KV_INFL) {
          asm volatile("s_waitcnt vmcnt(%0)" :: "i"(KV_INFL) : "memory");
          __builtin_amdgcn_s_barrier();
          if (tid == 448) st4s(rs, WS_CF1 + blockIdx.x * 128, tag);
        }
        if (i + 1 >= KV_INFL && i + 1 < 16) asm volatile("s_waitcnt vmcnt(%0)" :: "i"(KV_INFL - 1) : "memory");
      }
    }
    VMWAIT(0);
    v4u kf[16];
#pragma unroll
    for (int ss = 0; ss < 16; ++ss) kf[ss] = lds_ld<v4u>(L_KV + (16 * w + l15) * 1024 + (((ss + 16 * g) ^ l15) * 16));
#pragma unroll
    for (int ss = 0; ss < 16; ss += 4) asm volatile("" : "+v"(kf[ss]), "+v"(kf[ss + 1]), "+v"(kf[ss + 2]), "+v"(kf[ss + 3]));
    BAR();
    v2u vt[4][4][2];
    {
      const int q = l15 >> 2, pp = lane & 3;
#pragma unroll
      for (int t2 = 0; t2 < 2; ++t2) {
        const int row = 8 * g + 4 * t2 + q;
        const unsigned base = L_KV + row * 1024 + 8 * (pp & 1);
#pragma unroll
        for (int j = 0; j < 4; ++j) {
          const unsigned addr = base + (((8 * w + j + 4 * (pp >> 1)) ^ (row & 15)) * 16);
          const unsigned addr2 = addr + 65536;
          asm volatile("ds_read_b64_tr_b16 %0, %1" : "=v"(vt[0][j][t2]) : "v"(addr) : "memory");
          asm volatile("ds_read_b64_tr_b16 %0, %1 offset:32768" : "=v"(vt[1][j][t2]) : "v"(addr) : "memory");
          asm volatile("ds_read_b64_tr_b16 %0, %1" : "=v"(vt[2][j][t2]) : "v"(addr2) : "memory");
          asm volatile("ds_read_b64_tr_b16 %0, %1 offset:32768" : "=v"(vt[3][j][t2]) : "v"(addr2) : "memory");
        }
      }
    }
    if (w == 7) wait_flags<4>(rs, WS_CF1 + lane * 128, tag);
    BAR();
#pragma unroll
    for (int ks = 0; ks < 4; ++ks)
      asm volatile("" : "+v"(vt[ks][0][0]), "+v"(vt[ks][0][1]), "+v"(vt[ks][1][0]), "+v"(vt[ks][1][1]), "+v"(vt[ks][2][0]), "+v"(vt[ks][2][1]), "+v"(vt[ks][3][0]), "+v"(vt[ks][3][1]));
    if (isG) {
#pragma unroll
      for (int n = 0; n < 3; ++n) {
        const int kb = w + 8 * n;
        if (kb < 20) dma_kstep64(p.wg + (size_t)(32 * kb) * DF + 32 * gp, DF, lane, 16384 + kb * 2048);
      }
    }
    {
      unsigned spins = 0;
      v4u q[4];
      const int qoff = WS_QB + b * 32768 + w * 4096 + lane * 16;
      for (;;) {
        FENCE();
#pragma unroll
        for (int i = 0; i < 4; ++i) q[i] = __builtin_amdgcn_raw_buffer_load_b128(rs, qoff + i * 1024, 0, 16);
        if (wnone(bad16(q[0]) | bad16(q[1]) | bad16(q[2]) | bad16(q[3])) || ++spins > SPIN_CAP) break;
        __builtin_amdgcn_s_sleep(POLL_SLEEP);
      }
      const unsigned qb = L_Q + l15 * 2048 + (w >> 2) * 1024 + g * 256;
#pragma unroll
      for (int i = 0; i < 4; ++i) lds_st<v4u>(qb + ((((4 * w + i) & 15) ^ l15) * 16), q[i]);
    }
    BAR();
    {
      f32x4 acc[2] = {zero4, zero4};
#pragma unroll
      for (int hf = 0; hf < 2; ++hf) {
        v4u qf[8][2];
#pragma unroll
        for (int s8 = 0; s8 < 8; ++s8)
#pragma unroll
          for (int ht = 0; ht < 2; ++ht) qf[s8][ht] = lds_ld<v4u>(L_Q + (8 * hf + s8) * 2048 + ht * 1024 + g * 256 + ((l15 ^ (8 * hf + s8)) * 16));
        __builtin_amdgcn_sched_barrier(0);
#pragma unroll
        for (int s8 = 0; s8 < 8; ++s8) {
          acc[0] = mfma32(kf[8 * hf + s8], qf[s8][0], acc[0]);
          acc[1] = mfma32(kf[8 * hf + s8], qf[s8][1], acc[1]);
        }
        __builtin_amdgcn_sched_barrier(0);
      }
      float m0 = fmx(fmx3(acc[0][0], acc[0][1], acc[0][2]), acc[0][3]);
      float m1 = fmx(fmx3(acc[1][0], acc[1][1], acc[1][2]), acc[1][3]);
      m0 = x16_max(m0); m1 = x16_max(m1);
      m0 = x32_max(m0); m1 = x32_max(m1);
      lds_st<float>(L_MX + (l15 * 8 + w) * 4 + (g & 1) * 512, (g & 1) ? m1 : m0);
      BAR();
      const f32x4 a0 = lds_ld<f32x4>(L_MX + l15 * 32), c0 = lds_ld<f32x4>(L_MX + l15 * 32 + 16);
      const f32x4 a1 = lds_ld<f32x4>(L_MX + 512 + l15 * 32), c1 = lds_ld<f32x4>(L_MX + 512 + l15 * 32 + 16);
      const float mx0 = fmx(fmx3(fmx3(a0[0], a0[1], a0[2]), a0[3], c0[0]), fmx3(c0[1], c0[2], c0[3]));
      const float mx1 = fmx(fmx3(fmx3(a1[0], a1[1], a1[2]), a1[3], c1[0]), fmx3(c1[1], c1[2], c1[3]));
      float p0[4], p1[4];
#pragma unroll
      for (int e = 0; e < 4; ++e) { p0[e] = __builtin_amdgcn_exp2f(acc[0][e] - mx0); p1[e] = __builtin_amdgcn_exp2f(acc[1][e] - mx1); }
      const v2u pk0 = {pk2(p0[0], p0[1]), pk2(p0[2], p0[3])}, pk1 = {pk2(p1[0], p1[1]), pk2(p1[2], p1[3])};
      const unsigned pa = L_P + (((w >> 1)) * 64 + (2 * (w & 1) + (g >> 1)) * 16 + l15) * 16 + 8 * (g & 1);
      lds_st<v2u>(pa, pk0);
      lds_st<v2u>(pa + 4096, pk1);
      float sm0 = (bflo(pk0.x) + bfhi(pk0.x)) + (bflo(pk0.y) + bfhi(pk0.y));
      float sm1 = (bflo(pk1.x) + bfhi(pk1.x)) + (bflo(pk1.y) + bfhi(pk1.y));
      sm0 = x16_sum(sm0); sm1 = x16_sum(sm1);
      sm0 = x32_sum(sm0); sm1 = x32_sum(sm1);
      lds_st<float>(L_SM + (l15 * 8 + w) * 4 + (g & 1) * 512, (g & 1) ? sm1 : sm0);
      BAR();
      v4u pf[4][2];
#pragma unroll
      for (int ks = 0; ks < 4; ++ks)
#pragma unroll
        for (int ht = 0; ht < 2; ++ht) pf[ks][ht] = lds_ld<v4u>(L_P + ((ht * 4 + ks) * 64 + lane) * 16);
      __builtin_amdgcn_sched_barrier(0);
      if (w == 0) {
        const int hh = lane & 31;
        const f32x4 a = lds_ld<f32x4>(L_SM + hh * 32), c = lds_ld<f32x4>(L_SM + hh * 32 + 16);
        const f32x4 a2 = lds_ld<f32x4>(L_MX + hh * 32), c2 = lds_ld<f32x4>(L_MX + hh * 32 + 16);
        const float l = ((a[0] + a[1]) + (a[2] + a[3])) + ((c[0] + c[1]) + (c[2] + c[3]));
        const float m = fmaxf(fmaxf(fmaxf(a2[0], a2[1]), fmaxf(a2[2], a2[3])), fmaxf(fmaxf(c2[0], c2[1]), fmaxf(c2[2], c2[3])));
        if (lane < 32) st16s(rs, WS_ST2 + ((b * 32 + s) * 32 + lane) * 16, v4u{__float_as_uint(m), tag, __float_as_uint(l), tag});
      }
      f32x4 oc[4][2];
#pragma unroll
      for (int j = 0; j < 4; ++j) { oc[j][0] = zero4; oc[j][1] = zero4; }
#pragma unroll
      for (int ks = 0; ks < 4; ++ks)
#pragma unroll
        for (int j = 0; j < 4; ++j) {
          const v4u a = {vt[ks][j][0].x, vt[ks][j][0].y, vt[ks][j][1].x, vt[ks][j][1].y};
          oc[j][0] = mfma32(a, pf[ks][0], oc[j][0]);
          oc[j][1] = mfma32(a, pf[ks][1], oc[j][1]);
        }
      const bool odd = lane & 1;
      const int oeven = WS_OB + (((l15 & ~1) * 4 + (w >> 1)) * 128 + b * 32 + s) * 256 + ((w & 1) * 8 + g * 2 + (lane & 1)) * 16;
      unsigned ck[2];
#pragma unroll
      for (int ht = 0; ht < 2; ++ht) {
        v4u X, Y, S1, S2;
        X = v4u{pk2(oc[0][ht][0], oc[0][ht][1]), pk2(oc[0][ht][2], oc[0][ht][3]), pk2(oc[1][ht][0], oc[1][ht][1]), pk2(oc[1][ht][2], oc[1][ht][3])};
        Y = v4u{pk2(oc[2][ht][0], oc[2][ht][1]), pk2(oc[2][ht][2], oc[2][ht][3]), pk2(oc[3][ht][0], oc[3][ht][1]), pk2(oc[3][ht][2], oc[3][ht][3])};
#pragma unroll
        for (int e = 0; e < 4; ++e) {
          const unsigned T = odd ? X[e] : Y[e];
          const unsigned R = (unsigned)__builtin_amdgcn_mov_dpp((int)T, 0xB1, 0xf, 0xf, true);
          S1[e] = odd ? R : X[e];
          S2[e] = odd ? Y[e] : R;
        }
        __builtin_amdgcn_raw_buffer_store_b128(S1, rs, oeven, ht * (16 * 4 * 128 * 256), 16);
        __builtin_amdgcn_raw_buffer_store_b128(S2, rs, oeven + 4 * 128 * 256, ht * (16 * 4 * 128 * 256), 16);
        const unsigned f1 = (S1[0] ^ S1[1]) ^ (S1[2] ^ S1[3]), f2 = (S2[0] ^ S2[1]) ^ (S2[2] ^ S2[3]);
        const unsigned snd = odd ? f1 : f2;
        ck[ht] = (odd ? f2 : f1) ^ (unsigned)__builtin_amdgcn_mov_dpp((int)snd, 0xB1, 0xf, 0xf, true);
      }
      {
        auto s16 = __builtin_amdgcn_permlane16_swap(ck[0], ck[1], false, false);
        const unsigned v = s16[0] ^ s16[1];
        auto s32 = __builtin_amdgcn_permlane32_swap(v, v, false, false);
        const unsigned tot = s32[0] ^ s32[1] ^ tag;
        lds_st<unsigned>(12288 + ((lane & 31) * 8 + w) * 4, tot);
        BAR();
        if (w == 0) st16s(rs, WS_CK + (b * 32 + s) * 1024 + lane * 16, lds_ld<v4u>(12288 + lane * 16));
      }
    }
    BAR();
    if (!isG) {
      return;
    }
#pragma unroll
    for (int n = 0; n < 3; ++n) {
      const int kb = w + 8 * n;
      if (kb < 20) {
        dma_kstep64(p.wu + (size_t)(32 * kb) * DF + 32 * gp, DF, lane, L_WS + 40960 + kb * 2048);
      }
    }
    constexpr unsigned L_GA = 98304;
    constexpr unsigned L_RG = 119808;
    constexpr unsigned L_LN = 136192;
    constexpr unsigned L_HH = 136448;
    v4u fd[2][4];
    {
      const rsrc_t rs_wd = mkrs(p.wd, DF * DM * 2);
      v2u vd[2][8];
#pragma unroll
      for (int n = 0; n < 2; ++n) {
        const int u = min(w + 8 * n, 9);
        const int off = ((32 * gp + 8 * g) * DM + 64 * u + 4 * l15) * 2;
#pragma unroll
        for (int i = 0; i < 8; ++i) vd[n][i] = bl8(rs_wd, off, i * (DM * 2));
      }
#pragma unroll
      for (int n = 0; n < 2; ++n) tr8x4(vd[n], fd[n]);
    }
    v4u fg[3][4];
    VMWAIT(0);
#pragma unroll
    for (int n = 0; n < 3; ++n) {
      const int kb = min(w + 8 * n, 19);
      v2u tg[4], tu[4];
      tr_kstep64(L_WS + kb * 2048, lane, tg);
      tr_kstep64(L_WS + 40960 + kb * 2048, lane, tu);
      TRWAIT4(tg); TRWAIT4(tu);
      fg[n][0] = frag_of(tg, 0); fg[n][1] = frag_of(tg, 1); fg[n][2] = frag_of(tu, 0); fg[n][3] = frag_of(tu, 1);
    }
    const int gb_ = w >> 1;
    const rsrc_t rs_x = mkrs(p.x, NB * DM * 2);
    f32x4 ga4[2], cb4[2];
    const int m0 = 8 * (40 * (w & 1) + min(lane, 39));
    const bool live = lane < 40;
    const v4u x8v = bl16(rs_x, (gb_ * DM + m0) * 2, 0);
    {
      v4u a[2], c2[2], s2[2];
      unsigned spins = 0;
      for (;;) {
        FENCE();
        unsigned bad = 0;
#pragma unroll
        for (int n = 0; n < 2; ++n) {
          a[n] = ld16s(rs, WS_MP + (0 * 4 + gb_) * 2560 + m0 * 4 + n * 16);
          s2[n] = ld16s(rs, WS_MP + (2 * 4 + gb_) * 2560 + m0 * 4 + n * 16);
          c2[n] = ld16s(rs, WS_MP + (3 * 4 + gb_) * 2560 + m0 * 4 + n * 16);
        }
#pragma unroll
        for (int n = 0; n < 2; ++n) bad |= bad16(a[n]) | bad16(s2[n]) | bad16(c2[n]);
        if (wnone(bad) || ++spins > SPIN_CAP) break;
        __builtin_amdgcn_s_sleep(16);
      }
#pragma unroll
      for (int n = 0; n < 2; ++n) { ga4[n] = __builtin_bit_cast(f32x4, a[n]); cb4[n] = __builtin_bit_cast(f32x4, c2[n]); }
      const f32x4 sb0 = __builtin_bit_cast(f32x4, s2[0]), sb1 = __builtin_bit_cast(f32x4, s2[1]);
      lds_st<v4u>(L_GA + (4 * gb_ + 2) * 1296 + m0 * 2, v4u{pk2(cb4[0][0], cb4[0][1]), pk2(cb4[0][2], cb4[0][3]), pk2(cb4[1][0], cb4[1][1]), pk2(cb4[1][2], cb4[1][3])});
      lds_st<v4u>(L_GA + (4 * gb_ + 3) * 1296 + m0 * 2, v4u{pk2(sb0[0], sb0[1]), pk2(sb0[2], sb0[3]), pk2(sb1[0], sb1[1]), pk2(sb1[2], sb1[3])});
    }
    BAR();
    {
      f32x4 ac[4] = {zero4, zero4, zero4, zero4};
      v4u aa[3];
#pragma unroll
      for (int n = 0; n < 3; ++n) aa[n] = lds_ld<v4u>(L_GA + l15 * 1296 + (32 * min(w + 8 * n, 19) + 8 * g) * 2);
      __builtin_amdgcn_sched_barrier(0);
#pragma unroll
      for (int n = 0; n < 3; ++n) {
        if (w + 8 * n < 20) {
#pragma unroll
          for (int j = 0; j < 4; ++j) ac[j] = mfma32(aa[n], fg[n][j], ac[j]);
        }
      }
#pragma unroll
      for (int j = 0; j < 4; ++j) {
        lds_st<float>(L_RG + (((w * 2 + 0) * 4 + g) * 64 + 16 * j + l15) * 4, ac[j][2]);
        lds_st<float>(L_RG + (((w * 2 + 1) * 4 + g) * 64 + 16 * j + l15) * 4, ac[j][3]);
      }
    }
    BAR();
    float U1 = 0.f, U2 = 0.f;
    if (w < 4) {
#pragma unroll
      for (int ww = 0; ww < 8; ++ww) {
        U1 += lds_ld<float>(L_RG + (((ww * 2 + 0) * 4 + w) * 64 + lane) * 4);
        U2 += lds_ld<float>(L_RG + (((ww * 2 + 1) * 4 + w) * 64 + lane) * 4);
      }
    }
    BAR();
    {
      float s1 = 0.f, s2 = 0.f;
      v4u a[8];
      unsigned spins = 0;
      for (;;) {
        FENCE();
        unsigned bad = 0;
#pragma unroll
        for (int k8 = 0; k8 < 8; ++k8) a[k8] = __builtin_amdgcn_raw_buffer_load_b128(rs, WS_PO + gb_ * 1280 + m0 * 2, k8 * 5120, 16);
#pragma unroll
        for (int k8 = 0; k8 < 8; ++k8) bad |= bad16(a[k8]);
        if (wnone(bad) || ++spins > SPIN_CAP) break;
        __builtin_amdgcn_s_sleep(POLL_SLEEP);
      }
      float y[8];
#pragma unroll
      for (int q = 0; q < 4; ++q) {
        const float lo = ((bflo(a[0][q]) + bflo(a[1][q])) + (bflo(a[2][q]) + bflo(a[3][q]))) + ((bflo(a[4][q]) + bflo(a[5][q])) + (bflo(a[6][q]) + bflo(a[7][q])));
        const float hi = ((bfhi(a[0][q]) + bfhi(a[1][q])) + (bfhi(a[2][q]) + bfhi(a[3][q]))) + ((bfhi(a[4][q]) + bfhi(a[5][q])) + (bfhi(a[6][q]) + bfhi(a[7][q])));
        const float x0 = bflo(x8v[q]) + ga4[q >> 1][(2 * q) & 3] * lo, x1 = bfhi(x8v[q]) + ga4[q >> 1][(2 * q + 1) & 3] * hi;
        s1 += x0 + x1; s2 += x0 * x0 + x1 * x1;
        y[2 * q] = x0 * cb4[q >> 1][(2 * q) & 3]; y[2 * q + 1] = x1 * cb4[q >> 1][(2 * q + 1) & 3];
      }
      v4u yh, yl;
#pragma unroll
      for (int q = 0; q < 4; ++q) { yh[q] = pk2(y[2 * q], y[2 * q + 1]); yl[q] = pk2(y[2 * q] - bflo(yh[q]), y[2 * q + 1] - bfhi(yh[q])); }
      lds_st<v4u>(L_GA + (4 * gb_ + 0) * 1296 + m0 * 2, yh);
      lds_st<v4u>(L_GA + (4 * gb_ + 1) * 1296 + m0 * 2, yl);
      if (!live) { s1 = 0.f; s2 = 0.f; }
      BAR();
      f32x4 ac[4] = {zero4, zero4, zero4, zero4};
      v4u aa[3];
#pragma unroll
      for (int n = 0; n < 3; ++n) aa[n] = lds_ld<v4u>(L_GA + l15 * 1296 + (32 * min(w + 8 * n, 19) + 8 * g) * 2);
      __builtin_amdgcn_sched_barrier(0);
#pragma unroll
      for (int n = 0; n < 2; ++n)
#pragma unroll
        for (int j = 0; j < 4; ++j) ac[j] = mfma32(aa[n], fg[n][j], ac[j]);
      if (w < 4) {
#pragma unroll
        for (int j = 0; j < 4; ++j) ac[j] = mfma32(aa[2], fg[2][j], ac[j]);
      }
      __builtin_amdgcn_sched_barrier(0);
      s1 = x32_sum(x16_sum(sum16(s1)));
      s2 = x32_sum(x16_sum(sum16(s2)));
      if (lane == 0) lds_st<v2u>(L_LN + w * 8, v2u{__float_as_uint(s1), __float_as_uint(s2)});
#pragma unroll
      for (int j = 0; j < 4; ++j) lds_st<float>(L_RG + (((w * 2 + 0) * 4 + g) * 64 + 16 * j + l15) * 4, ac[j][0] + ac[j][1]);
    }
    BAR();
    if (w < 4) {
      float v[8];
#pragma unroll
      for (int ww = 0; ww < 8; ++ww) v[ww] = lds_ld<float>(L_RG + (((ww * 2 + 0) * 4 + w) * 64 + lane) * 4);
      const v4u ln = lds_ld<v4u>(L_LN + w * 16);
      const float T = ((v[0] + v[1]) + (v[2] + v[3])) + ((v[4] + v[5]) + (v[6] + v[7]));
      const float s1 = __uint_as_float(ln.x) + __uint_as_float(ln.z), s2 = __uint_as_float(ln.y) + __uint_as_float(ln.w);
      const float mean = s1 * (1.f / DM);
      const float rstd = rsqrtf(fmaxf(s2 * (1.f / DM) - mean * mean, 0.f) + 1e-5f);
      const float val = rstd * (T - mean * U1) + U2;
      auto sw = __builtin_amdgcn_permlane32_swap(__float_as_uint(val), __float_as_uint(val), false, false);
      const float gv = __uint_as_float(sw[0]), uv = __uint_as_float(sw[1]);
      const float z = 0.7978845608028654f * (gv + 0.044715f * gv * gv * gv);
      const float th = 1.f - 2.f * __builtin_amdgcn_rcpf(1.f + __expf(2.f * z));
      const float hh = 0.5f * gv * (1.f + th) * uv;
      const unsigned hi = pk2(hh, 0.f) & 0xFFFFu;
      const unsigned lo = pk2(hh - bf2f(hi), 0.f) & 0xFFFFu;
      lds_st<u16>(L_HH + (4 * w + (lane >> 5)) * 80 + (lane & 31) * 2, (u16)(lane < 32 ? hi : lo));
    }
    BAR();
    {
      const v4u a = lds_ld<v4u>(L_HH + l15 * 80 + g * 16);
#pragma unroll
      for (int n = 0; n < 2; ++n) {
        const int u = w + 8 * n;
        if (u < 10) {
          f32x4 o;
#pragma unroll
          for (int j = 0; j < 4; ++j) { const f32x4 d = mfma32(a, fd[n][j], zero4); o[j] = d[0] + d[1]; }
          const v4u ov = {nosent(__float_as_uint(o[0])), nosent(__float_as_uint(o[1])), nosent(__float_as_uint(o[2])), nosent(__float_as_uint(o[3]))};
          st16s(rs, WS_PD + (8 * u + (l15 >> 1)) * 6912 + gp * 128 + g * 32 + (l15 & 1) * 16, ov);
        }
      }
    }
  }
}

extern "C" void kernel_launch(void* const* d_in, const int* in_sizes, int n_in, void* d_out, int out_size, void* d_ws, size_t ws_size, hipStream_t stream) {
  Params p;
  p.x = (const u16*)d_in[0]; p.ckv = (const u16*)d_in[1]; p.wq = (const u16*)d_in[2]; p.wuk = (const u16*)d_in[3];
  p.wuv = (const u16*)d_in[4]; p.wo = (const u16*)d_in[5]; p.wg = (const u16*)d_in[7]; p.wu = (const u16*)d_in[8];
  p.wd = (const u16*)d_in[9]; p.c = (const u16*)d_in[10]; p.wmod = (const u16*)d_in[11];
  p.out = (u16*)d_out; p.ws = (unsigned char*)d_ws;
  (void)hipFuncSetAttribute((const void*)mega, hipFuncAttributeMaxDynamicSharedMemorySize, LDS_BYTES);
  hipLaunchKernelGGL(mega, dim3(256), dim3(512), LDS_BYTES, stream, p);
}
```
